```python
import torch

def _softmax(x, axis=-1):
    return torch.softmax(x, dim=axis)
def _gelu(x):
    return torch.nn.functional.gelu(x, approximate="tanh")
def _silu(x):
    return torch.nn.functional.silu(x)
def _sigmoid(x):
    return torch.sigmoid(x)
def _layernorm(x, axis=-1, eps=1e-5):
    m = x.mean(dim=axis, keepdim=True)
    v = x.var(dim=axis, keepdim=True, unbiased=False)
    return (x - m) / torch.sqrt(v + eps)
def _scatter_add(tgt, idx, src, axis=0):
    return tgt.clone().index_add_(axis, idx, src)
def _segment_sum(src, seg, n):
    out = torch.zeros((n,) + src.shape[1:], dtype=src.dtype)
    return out.index_add_(0, seg, src)
def _pad(x, pw):
    flat = tuple(v for pair in reversed(pw) for v in pair)
    return torch.nn.functional.pad(x, flat)

kernel_name = 'interleave_layers__grouped_kv_mqa__glu_gated_bilinear__prompt'

def setup_inputs():

    _w = (lambda a: (lambda t: t.to(torch.bfloat16) if t.is_floating_point() else t)(torch.as_tensor(a)))
    def _A_setup():
        B, S, D, H = 1, 2048, 448, 16
        Dh = D // H
        return {
            "x": _w((lambda *s: torch.randn(*s, dtype=torch.bfloat16) * 0.05)(B, S, D)),
            "wq": _w((lambda *s: torch.randn(*s, dtype=torch.bfloat16) * 0.05)(D, H * Dh)),
            "wk": _w((lambda *s: torch.randn(*s, dtype=torch.bfloat16) * 0.05)(D, Dh)),
            "wv": _w((lambda *s: torch.randn(*s, dtype=torch.bfloat16) * 0.05)(D, Dh)),
            "wo": _w((lambda *s: torch.randn(*s, dtype=torch.bfloat16) * 0.05)(H * Dh, D)),
        }
    _A_in = _A_setup()

    def _B_setup():
        B, S, D, F = 1, 2048, 448, 1792
        return {
            "x": _w((lambda *s: torch.randn(*s, dtype=torch.bfloat16) * 0.05)(B, S, D)),
            "w_gate": _w((lambda *s: torch.randn(*s, dtype=torch.bfloat16) * 0.4)(D, F) * (D ** -0.5)),
            "w_up": _w((lambda *s: torch.randn(*s, dtype=torch.bfloat16) * 0.4)(D, F) * (D ** -0.5)),
            "w_down": _w((lambda *s: torch.randn(*s, dtype=torch.bfloat16) * 0.4)(F, D) * (F ** -0.5)),
        }
    _B_in = _B_setup()

    return {
        "A_x": _A_in["x"],
        "A_wq": _A_in["wq"],
        "A_wk": _A_in["wk"],
        "A_wv": _A_in["wv"],
        "A_wo": _A_in["wo"],
        "B_x": _B_in["x"],
        "B_w_gate": _B_in["w_gate"],
        "B_w_up": _B_in["w_up"],
        "B_w_down": _B_in["w_down"],
    }

def reference(A_x, A_wq, A_wk, A_wv, A_wo, B_x, B_w_gate, B_w_up, B_w_down):
    def _A_ref(x, wq, wk, wv, wo):
        B, S, D = x.shape
        H = 16
        Dh = D // H
        q = (lambda x, *d: x.permute(*d))((x @ wq).reshape(B, S, H, Dh), 0, 2, 1, 3)
        k = x @ wk
        v = x @ wv
        scores = torch.einsum("bhqd,bkd->bhqk", q, k) * (Dh ** -0.5)
        p = _softmax(scores, axis=-1)
        o = torch.einsum("bhqk,bkd->bhqd", p, v)
        return (lambda x, *d: x.permute(*d))(o, 0, 2, 1, 3).reshape(B, S, H * Dh) @ wo

    def _B_ref(x, w_gate, w_up, w_down):
        g = torch.einsum("bsd,df->bsf", x, w_gate)
        u = torch.einsum("bsd,df->bsf", x, w_up)
        h = g * u
        return torch.einsum("bsf,fd->bsd", h, w_down)

    x = A_x
    x = x + _A_ref(_layernorm(x, axis=-1), A_wq, A_wk, A_wv, A_wo)
    x = x + _B_ref(_layernorm(x, axis=-1), B_w_gate, B_w_up, B_w_down)
    x = x + _A_ref(_layernorm(x, axis=-1), A_wq, A_wk, A_wv, A_wo)
    x = x + _B_ref(_layernorm(x, axis=-1), B_w_gate, B_w_up, B_w_down)
    return x
```

```cpp
#include <hip/hip_runtime.h>
#include <hip/hip_bf16.h>
#include <stdint.h>

typedef unsigned u32;
typedef u32 u32x2 __attribute__((ext_vector_type(2)));
typedef u32 u32x4 __attribute__((ext_vector_type(4)));
typedef float f32x2 __attribute__((ext_vector_type(2)));
typedef float f32x4 __attribute__((ext_vector_type(4)));
typedef float f32x8 __attribute__((ext_vector_type(8)));
typedef float f32x16 __attribute__((ext_vector_type(16)));
typedef __bf16 bf16x2 __attribute__((ext_vector_type(2)));
typedef __bf16 bf16x8 __attribute__((ext_vector_type(8)));
typedef __bf16 bf16x16 __attribute__((ext_vector_type(16)));
#define DI __device__ __forceinline__

constexpr int S_ = 2048, D_ = 448, F_ = 1792, DH = 28;
constexpr size_t KB = 1024;
constexpr size_t OFF_WQ = 0;
constexpr size_t OFF_WKV = OFF_WQ + 448 * KB;
constexpr size_t OFF_WO = OFF_WKV + 56 * KB;
constexpr size_t OFF_WGU = OFF_WO + 512 * KB;
constexpr size_t OFF_WD = OFF_WGU + 3136 * KB;
constexpr size_t OFF_CS = OFF_WD + 1792 * KB;
constexpr size_t OFF_XF = OFF_CS + 32 * KB;
constexpr size_t OFF_QF = OFF_XF + 4 * 1792 * KB;
constexpr size_t OFF_KV = OFF_QF + 2048 * KB;
constexpr size_t OFF_KN = OFF_KV + 256 * KB;
constexpr size_t OFF_OF = OFF_KN + 4 * KB;
constexpr size_t OFF_HF = OFF_OF + 2048 * KB;
constexpr size_t WS_END = OFF_HF + 7168 * KB;

DI f32x16 mfma(u32x4 a, u32x4 b, f32x16 c) {
    return __builtin_amdgcn_mfma_f32_32x32x16_bf16(__builtin_bit_cast(bf16x8, a), __builtin_bit_cast(bf16x8, b), c, 0, 0, 0);
}
DI f32x16 smfmac_hi(u32x4 a, f32x8 b, f32x16 c) {
    return __builtin_amdgcn_smfmac_f32_32x32x32_bf16(__builtin_bit_cast(bf16x8, a), __builtin_bit_cast(bf16x16, b), c, 0xDDDD, 0, 0);
}
DI u32 pk(float lo, float hi) { f32x2 v = {lo, hi}; return __builtin_bit_cast(u32, __builtin_convertvector(v, bf16x2)); }
DI float dot2(u32 a, u32 b, float c) { return __builtin_amdgcn_fdot2_f32_bf16(__builtin_bit_cast(bf16x2, a), __builtin_bit_cast(bf16x2, b), c, false); }
DI float blo(u32 x) { return __uint_as_float(x << 16); }
DI float bhi(u32 x) { return __uint_as_float(x & 0xffff0000u); }
DI float xhalf_sum(float v) { auto r = __builtin_amdgcn_permlane32_swap(__float_as_uint(v), __float_as_uint(v), false, false); return __uint_as_float(r[0]) + __uint_as_float(r[1]); }
DI float xhalf_max(float v) { auto r = __builtin_amdgcn_permlane32_swap(__float_as_uint(v), __float_as_uint(v), false, false); return fmaxf(__uint_as_float(r[0]), __uint_as_float(r[1])); }
DI u32x4 pack8(const f32x16& x, int s) { return (u32x4){pk(x[8 * s], x[8 * s + 1]), pk(x[8 * s + 2], x[8 * s + 3]), pk(x[8 * s + 4], x[8 * s + 5]), pk(x[8 * s + 6], x[8 * s + 7])}; }
DI int prow(int h, int j) { return 8 * (j >> 2) + 4 * h + (j & 3); }
DI int feat(int ks, int h, int j) {
    const int p = ks / 7, s = ks % 7;
    if (s < 6) return 112 * p + (s >= 3 ? 56 + 16 * (s - 3) : 16 * s) + prow(h, j);
    return 112 * p + 56 * (j >> 2) + 48 + 4 * h + (j & 3);
}

__global__ void __launch_bounds__(256) k_prep(const uint16_t* x, const uint16_t* wq, const uint16_t* wk, const uint16_t* wv, const uint16_t* wo,
                                              const uint16_t* wg, const uint16_t* wu, const uint16_t* wd, char* ws) {
    __shared__ float red[4][32];
    const int job = blockIdx.x, w = threadIdx.x >> 6, lane = threadIdx.x & 63, m = lane & 31, h = lane >> 5;
    if (job < 130) {
        const uint16_t* src; int pitch; bool valid; char* dst;
        if (job < 16) { src = wq + job * 28 + m; pitch = D_; valid = m < 28; dst = ws + OFF_WQ + (size_t)job * 28 * KB; }
        else if (job < 18) { src = (job == 16 ? wk : wv) + m; pitch = DH; valid = m < 28; dst = ws + OFF_WKV + (size_t)(job - 16) * 28 * KB; }
        else { const int t = job - 18; src = (m < 16 ? wg : wu) + 16 * t + (m & 15); pitch = F_; valid = true; dst = ws + OFF_WGU + (size_t)t * 28 * KB; }
        float cs = 0.f;
        for (int i = 0; i < 7; ++i) {
            const int ks = 7 * w + i; u32 e[8];
#pragma unroll
            for (int j = 0; j < 8; ++j) e[j] = valid ? src[(size_t)feat(ks, h, j) * pitch] : 0;
            u32x4 v = {e[0] | (e[1] << 16), e[2] | (e[3] << 16), e[4] | (e[5] << 16), e[6] | (e[7] << 16)};
#pragma unroll
            for (int j = 0; j < 4; ++j) cs = dot2(v[j], 0x3f803f80u, cs);
            *(u32x4*)(dst + ks * KB + lane * 16) = v;
        }
        cs = xhalf_sum(cs);
        if (h == 0) red[w][m] = cs;
        __syncthreads();
        if (threadIdx.x < 32) ((float*)(ws + OFF_CS))[job * 32 + m] = (red[0][m] + red[1][m]) + (red[2][m] + red[3][m]);
    } else if (job < 146) {
        const int q = job - 130, j8 = q >> 1, mt = q & 1, lf = 32 * mt + m;
        for (int i = 0; i < 8; ++i) {
            const int ks = 8 * w + i, head = ks >> 1; u32 e[8];
#pragma unroll
            for (int j = 0; j < 8; ++j) { const int d = 16 * (ks & 1) + prow(h, j); e[j] = (lf < 56 && d < 28) ? wo[(size_t)(head * 28 + d) * D_ + 56 * j8 + lf] : 0; }
            *(u32x4*)(ws + OFF_WO + (size_t)(q * 32 + ks) * KB + lane * 16) = (u32x4){e[0] | (e[1] << 16), e[2] | (e[3] << 16), e[4] | (e[5] << 16), e[6] | (e[7] << 16)};
        }
    } else if (job < 210) {
        const int q = job - 146, jm = q >> 2, j8 = jm >> 1, mt = jm & 1, lf = 32 * mt + m;
        for (int i = 0; i < 7; ++i) {
            const int ks = 28 * (q & 3) + 7 * w + i; u32 e[8];
#pragma unroll
            for (int j = 0; j < 8; ++j) e[j] = lf < 56 ? wd[(size_t)(16 * ks + prow(h, j)) * D_ + 56 * j8 + lf] : 0;
            *(u32x4*)(ws + OFF_WD + (size_t)(jm * 112 + ks) * KB + lane * 16) = (u32x4){e[0] | (e[1] << 16), e[2] | (e[3] << 16), e[4] | (e[5] << 16), e[6] | (e[7] << 16)};
        }
    } else {
        const int n = job - 210;
        for (int i = 0; i < 7; ++i) {
            const int ks = 7 * w + i;
            const uint16_t* r = x + (size_t)(32 * n + m) * D_;
            const u32x2 a = *(const u32x2*)(r + feat(ks, h, 0)), b = *(const u32x2*)(r + feat(ks, h, 4));
            *(u32x4*)(ws + OFF_XF + (size_t)(n * 28 + ks) * KB + lane * 16) = (u32x4){a[0], a[1], b[0], b[1]};
        }
    }
}

DI void ln_stats(float s1, float s2, float& mean, float& rstd) {
    s1 = xhalf_sum(s1); s2 = xhalf_sum(s2);
    mean = s1 * (1.f / D_);
    const float var = fmaxf(s2 * (1.f / D_) - mean * mean, 0.f);
    rstd = __builtin_amdgcn_rsqf(var + 1e-5f);
}

__global__ void __launch_bounds__(512) k_qkv(char* ws, int xsel) {
    __shared__ float st[2][32];
    const int b = blockIdx.x, g = (b & 7) * 4 + (b >> 6), j8 = (b >> 3) & 7;
    const int w = __builtin_amdgcn_readfirstlane(threadIdx.x >> 6), lane = threadIdx.x & 63, m = lane & 31, h = lane >> 5;
    if (w > 4 || (w == 4 && j8 >= 4)) return;
    const bool isq = w < 4, isv = !isq && (j8 >> 1);
    const int n = isq ? (w & 1) : (j8 & 1), tile = isq ? (2 * j8 + (w >> 1)) : (16 + (j8 >> 1));
    const char* wa = ws + OFF_WQ + (size_t)tile * 28 * KB + lane * 16;
    const char* xb = ws + OFF_XF + ((size_t)xsel * 64 + 2 * g + n) * 28 * KB + lane * 16;
    f32x16 acc = {}; float s1 = 0.f, s2 = 0.f;
#pragma unroll 4
    for (int ks = 0; ks < 28; ++ks) {
        const u32x4 a = *(const u32x4*)(wa + ks * KB), xx = *(const u32x4*)(xb + ks * KB);
        acc = isv ? mfma(xx, a, acc) : mfma(a, xx, acc);
#pragma unroll
        for (int i = 0; i < 4; ++i) { s1 = dot2(xx[i], 0x3f803f80u, s1); s2 = dot2(xx[i], xx[i], s2); }
    }
    float mean, rstd; ln_stats(s1, s2, mean, rstd);
    const float* cs = (const float*)(ws + OFF_CS) + tile * 32;
    if (!isv) {
        const float sc = isq ? rstd * (0.18898223650461362f * 1.4426950408889634f) : rstd;
#pragma unroll
        for (int r = 0; r < 16; ++r) acc[r] = (acc[r] - mean * cs[(r & 3) + 8 * (r >> 2) + 4 * h]) * sc;
        if (isq) {
            char* q = ws + OFF_QF + ((size_t)(g * 8 + j8) * 4 + (w >> 1) * 2 + n) * 2 * KB + lane * 16;
            *(u32x4*)q = pack8(acc, 0); *(u32x4*)(q + KB) = pack8(acc, 1);
        } else {
            u32x4 k0 = pack8(acc, 0), k1 = pack8(acc, 1);
            float nn = 0.f;
#pragma unroll
            for (int i = 0; i < 4; ++i) { nn = dot2(k0[i], k0[i], nn); nn = dot2(k1[i], k1[i], nn); }
            nn = xhalf_sum(nn);
#pragma unroll
            for (int o = 16; o >= 1; o >>= 1) nn = fmaxf(nn, __shfl_xor(nn, o));
            if (h == 1) k1[2] = 0x3f803f80u;
            const int slot = (m & 19) | ((m & 4) << 1) | ((m & 8) >> 1);
            char* kp = ws + OFF_KV + (size_t)(2 * g + n) * 4 * KB + (slot + 32 * h) * 16;
            *(u32x4*)kp = k0; *(u32x4*)(kp + KB) = k1;
            if (lane == 0) ((float*)(ws + OFF_KN))[2 * g + n] = nn;
        }
    } else {
        if (h == 0) { st[0][m] = mean; st[1][m] = rstd; }
        __builtin_amdgcn_fence(__ATOMIC_RELEASE, "wavefront"); __builtin_amdgcn_wave_barrier(); __builtin_amdgcn_fence(__ATOMIC_ACQUIRE, "wavefront");
        const float cv = cs[m];
#pragma unroll
        for (int r = 0; r < 16; ++r) { const int row = (r & 3) + 8 * (r >> 2) + 4 * h; acc[r] = m == 28 ? 1.f : (acc[r] - st[0][row] * cv) * st[1][row]; }
        char* vp = ws + OFF_KV + ((size_t)(2 * g + n) * 4 + 2) * KB + lane * 16;
        *(u32x4*)vp = pack8(acc, 0); *(u32x4*)(vp + KB) = pack8(acc, 1);
    }
}

__global__ void __launch_bounds__(512) k_attn(char* ws) {
    extern __shared__ __attribute__((aligned(16))) char lds[];
    const int b = blockIdx.x, g = (b & 7) * 4 + (b >> 6), j8 = (b >> 3) & 7;
    const int w = __builtin_amdgcn_readfirstlane(threadIdx.x >> 6), lane = threadIdx.x & 63, m = lane & 31, h = lane >> 5;
    const char* kv = ws + OFF_KV + (size_t)w * 32 * KB + lane * 16;
    u32x4 q[4][2];
    { const char* qp = ws + OFF_QF + (size_t)(g * 8 + j8) * 8 * KB + lane * 16;
#pragma unroll
      for (int i = 0; i < 8; ++i) q[i >> 1][i & 1] = *(const u32x4*)(qp + i * KB); }
    float kn = ((const float*)(ws + OFF_KN))[lane];
    u32x4 kf[2][4];
#pragma unroll
    for (int i = 0; i < 4; ++i) kf[0][i] = *(const u32x4*)(kv + i * KB);
#pragma unroll
    for (int o = 32; o >= 1; o >>= 1) kn = fmaxf(kn, __shfl_xor(kn, o));
    bool safe = true;
    float sh[4];
#pragma unroll
    for (int t = 0; t < 4; ++t) {
        float qq = 0.f;
#pragma unroll
        for (int i = 0; i < 4; ++i) { qq = dot2(q[t][0][i], q[t][0][i], qq); qq = dot2(q[t][1][i], q[t][1][i], qq); }
        qq = xhalf_sum(qq);
        sh[t] = __builtin_amdgcn_sqrtf(qq * kn) * 1.01f;
        safe = safe && (sh[t] <= 48.f);
    }
    if (!__all(safe)) {
        float* mx = (float*)lds;
        float rm[4] = {-INFINITY, -INFINITY, -INFINITY, -INFINITY};
#pragma unroll 1
        for (int kt = 0; kt < 8; ++kt) {
            const u32x4 k0 = *(const u32x4*)(kv + kt * 4 * KB), k1 = *(const u32x4*)(kv + kt * 4 * KB + KB);
#pragma unroll
            for (int t = 0; t < 4; ++t) {
                f32x16 s = {}; s = mfma(k0, q[t][0], s); s = mfma(k1, q[t][1], s);
#pragma unroll
                for (int r = 0; r < 16; ++r) rm[t] = fmaxf(rm[t], s[r]);
            }
        }
#pragma unroll
        for (int t = 0; t < 4; ++t) { rm[t] = xhalf_max(rm[t]); if (h == 0) mx[(w * 4 + t) * 32 + m] = rm[t]; }
        __syncthreads();
#pragma unroll
        for (int t = 0; t < 4; ++t) { float v = mx[t * 32 + m];
#pragma unroll
            for (int u = 1; u < 8; ++u) v = fmaxf(v, mx[(u * 4 + t) * 32 + m]);
            sh[t] = v; }
        __syncthreads();
    }
#pragma unroll
    for (int t = 0; t < 4; ++t) {
        const u32 hi = pk(-sh[t], 0.f) & 0xffffu; const u32 lo = pk(-sh[t] - blo(hi), 0.f);
        if (h == 1) q[t][1][2] = hi | (lo << 16);
    }
    f32x16 o[4] = {};
    f32x16 sb[3];
    const f32x16 zero = {};
    kf[1][0] = *(const u32x4*)(kv + 4 * KB);
    sb[0] = mfma(kf[0][0], q[0][0], zero); sb[0] = mfma(kf[0][1], q[0][1], sb[0]);
#define SB() __builtin_amdgcn_sched_barrier(0)
#define EX4(S, i) do { S[i] = __builtin_amdgcn_exp2f(S[i]); S[i + 1] = __builtin_amdgcn_exp2f(S[i + 1]); S[i + 2] = __builtin_amdgcn_exp2f(S[i + 2]); S[i + 3] = __builtin_amdgcn_exp2f(S[i + 3]); SB(); } while (0)
    SB();
#pragma unroll
    for (int u = 0; u < 32; ++u) {
        const int kt = u >> 2, t = u & 3, un = u + 1, ktn = un >> 2, tn = un & 3, up = u - 1;
        f32x16& cur = sb[u % 3]; f32x16& nxt = sb[un % 3]; f32x16& prv = sb[(u + 2) % 3];
        if (un < 32) nxt = mfma(kf[ktn & 1][0], q[tn][0], zero);
        SB(); EX4(cur, 0);
        if (un < 32) nxt = mfma(kf[ktn & 1][1], q[tn][1], nxt);
        SB(); EX4(cur, 4);
        if (up >= 0) o[up & 3] = smfmac_hi(kf[(up >> 2) & 1][3], __builtin_shufflevector(prv, prv, 8, 9, 10, 11, 12, 13, 14, 15), o[up & 3]);
        SB();
        if (t == 3) { if (kt + 2 < 8) kf[kt & 1][0] = *(const u32x4*)(kv + (kt + 2) * 4 * KB); }
        else if (kt + 1 < 8) kf[(kt + 1) & 1][t + 1] = *(const u32x4*)(kv + ((kt + 1) * 4 + t + 1) * KB);
        SB(); EX4(cur, 8);
        o[t] = smfmac_hi(kf[kt & 1][2], __builtin_shufflevector(cur, cur, 0, 1, 2, 3, 4, 5, 6, 7), o[t]);
        SB(); EX4(cur, 12);
    }
    o[3] = smfmac_hi(kf[1][3], __builtin_shufflevector(sb[31 % 3], sb[31 % 3], 8, 9, 10, 11, 12, 13, 14, 15), o[3]);
#pragma unroll
    for (int c = 0; c < 8; ++c) {
        const f32x16& x = o[c >> 1]; const int s = 8 * (c & 1);
        *(f32x4*)(lds + ((w * 8 + c) * 2) * KB + lane * 16) = (f32x4){x[s], x[s + 1], x[s + 2], x[s + 3]};
        *(f32x4*)(lds + ((w * 8 + c) * 2 + 1) * KB + lane * 16) = (f32x4){x[s + 4], x[s + 5], x[s + 6], x[s + 7]};
    }
    __syncthreads();
    f32x4 a0 = {}, a1 = {}; float l = 0.f;
#pragma unroll
    for (int u = 0; u < 8; ++u) {
        a0 += *(const f32x4*)(lds + ((u * 8 + w) * 2) * KB + lane * 16);
        a1 += *(const f32x4*)(lds + ((u * 8 + w) * 2 + 1) * KB + lane * 16);
        l += *(const float*)(lds + ((u * 8 + (w | 1)) * 2 + 1) * KB + (32 + m) * 16);
    }
    const float il = __builtin_amdgcn_rcpf(l);
    const int t = w >> 1, head = 2 * j8 + (t >> 1), n = t & 1;
    *(u32x4*)(ws + OFF_OF + ((size_t)(2 * g + n) * 32 + 2 * head + (w & 1)) * KB + lane * 16) =
        (u32x4){pk(a0[0] * il, a0[1] * il), pk(a0[2] * il, a0[3] * il), pk(a1[0] * il, a1[1] * il), pk(a1[2] * il, a1[3] * il)};
}

DI void resid_store(const f32x16& acc, const char* xin, char* xout, uint16_t* out, int nt, int j8, int mt, int lane) {
    const int m = lane & 31, h = lane >> 5;
    const int ks0 = 7 * (j8 >> 1) + 3 * (j8 & 1), ksh = 7 * (j8 >> 1) + 6;
    const size_t base = (size_t)nt * 28 * KB + lane * 16;
    u32x4 r0, r1;
    if (mt == 0) { r0 = *(const u32x4*)(xin + base + ks0 * KB); r1 = *(const u32x4*)(xin + base + (ks0 + 1) * KB); }
    else { r0 = *(const u32x4*)(xin + base + (ks0 + 2) * KB); const u32x2 t = *(const u32x2*)(xin + base + ksh * KB + 8 * (j8 & 1)); r1 = (u32x4){t[0], t[1], 0u, 0u}; }
    u32x4 p0, p1;
#pragma unroll
    for (int i = 0; i < 4; ++i) {
        p0[i] = pk(acc[2 * i] + blo(r0[i]), acc[2 * i + 1] + bhi(r0[i]));
        p1[i] = pk(acc[8 + 2 * i] + blo(r1[i]), acc[8 + 2 * i + 1] + bhi(r1[i]));
    }
    if (out == nullptr) {
        if (mt == 0) { *(u32x4*)(xout + base + ks0 * KB) = p0; *(u32x4*)(xout + base + (ks0 + 1) * KB) = p1; }
        else { *(u32x4*)(xout + base + (ks0 + 2) * KB) = p0; *(u32x2*)(xout + base + ksh * KB + 8 * (j8 & 1)) = (u32x2){p1[0], p1[1]}; }
    } else {
        uint16_t* r = out + (size_t)(32 * nt + m) * D_ + 56 * j8 + 32 * mt + 4 * h;
        *(u32x2*)r = (u32x2){p0[0], p0[1]}; *(u32x2*)(r + 8) = (u32x2){p0[2], p0[3]}; *(u32x2*)(r + 16) = (u32x2){p1[0], p1[1]};
        if (mt == 0) *(u32x2*)(r + 24) = (u32x2){p1[2], p1[3]};
    }
}

__global__ void __launch_bounds__(256) k_oproj(char* ws, int xin, int xout) {
    const int b = blockIdx.x, g = (b & 7) * 4 + (b >> 6), j8 = (b >> 3) & 7;
    const int w = __builtin_amdgcn_readfirstlane(threadIdx.x >> 6), lane = threadIdx.x & 63, mt = w >> 1, n = w & 1;
    const char* wa = ws + OFF_WO + (size_t)(j8 * 2 + mt) * 32 * KB + lane * 16;
    const char* ob = ws + OFF_OF + (size_t)(2 * g + n) * 32 * KB + lane * 16;
    f32x16 acc = {};
#pragma unroll 8
    for (int ks = 0; ks < 32; ++ks) acc = mfma(*(const u32x4*)(wa + ks * KB), *(const u32x4*)(ob + ks * KB), acc);
    resid_store(acc, ws + OFF_XF + (size_t)xin * 1792 * KB, ws + OFF_XF + (size_t)xout * 1792 * KB, nullptr, 2 * g + n, j8, mt, lane);
}

__global__ void __launch_bounds__(256) k_gemm1(char* ws, int xsel) {
    const int b = blockIdx.x, blk = (b & 7) * 2 + (b >> 7), i16 = (b >> 3) & 15;
    const int w = __builtin_amdgcn_readfirstlane(threadIdx.x >> 6), lane = threadIdx.x & 63, h = lane >> 5;
    const int nt = 4 * blk + w;
    const char* xb = ws + OFF_XF + ((size_t)xsel * 64 + nt) * 28 * KB + lane * 16;
    u32x4 x[28]; float s1 = 0.f, s2 = 0.f;
#pragma unroll
    for (int ks = 0; ks < 28; ++ks) x[ks] = *(const u32x4*)(xb + ks * KB);
#pragma unroll
    for (int ks = 0; ks < 28; ++ks)
#pragma unroll
        for (int i = 0; i < 4; ++i) { s1 = dot2(x[ks][i], 0x3f803f80u, s1); s2 = dot2(x[ks][i], x[ks][i], s2); }
    float mean, rstd; ln_stats(s1, s2, mean, rstd);
    const float r2 = rstd * rstd;
    for (int tt = 0; tt < 7; ++tt) {
        const int t = 7 * i16 + tt;
        const char* wa = ws + OFF_WGU + (size_t)t * 28 * KB + lane * 16;
        f32x16 acc = {};
#pragma unroll
        for (int ks = 0; ks < 28; ++ks) acc = mfma(*(const u32x4*)(wa + ks * KB), x[ks], acc);
        const float* cs = (const float*)(ws + OFF_CS) + (18 + t) * 32 + 4 * h;
        float hh[8];
#pragma unroll
        for (int r = 0; r < 8; ++r) { const int row = (r & 3) + 8 * (r >> 2); hh[r] = (acc[r] - mean * cs[row]) * (acc[r + 8] - mean * cs[row + 16]) * r2; }
        *(u32x4*)(ws + OFF_HF + ((size_t)nt * 112 + t) * KB + lane * 16) = (u32x4){pk(hh[0], hh[1]), pk(hh[2], hh[3]), pk(hh[4], hh[5]), pk(hh[6], hh[7])};
    }
}

__global__ void __launch_bounds__(256) k_gemm2(char* ws, int xin, int xout, uint16_t* out) {
    const int b = blockIdx.x, g = (b & 7) * 4 + (b >> 6), j8 = (b >> 3) & 7;
    const int w = __builtin_amdgcn_readfirstlane(threadIdx.x >> 6), lane = threadIdx.x & 63, mt = w >> 1, n = w & 1;
    const char* wa = ws + OFF_WD + (size_t)(j8 * 2 + mt) * 112 * KB + lane * 16;
    const char* hb = ws + OFF_HF + (size_t)(2 * g + n) * 112 * KB + lane * 16;
    f32x16 acc = {};
#pragma unroll 8
    for (int ks = 0; ks < 112; ++ks) acc = mfma(*(const u32x4*)(wa + ks * KB), *(const u32x4*)(hb + ks * KB), acc);
    resid_store(acc, ws + OFF_XF + (size_t)xin * 1792 * KB, ws + OFF_XF + (size_t)xout * 1792 * KB, out, 2 * g + n, j8, mt, lane);
}

extern "C" void kernel_launch(void* const* d_in, const int* in_sizes, int n_in, void* d_out, int out_size, void* d_ws, size_t ws_size, hipStream_t stream) {
    const uint16_t* x = (const uint16_t*)d_in[0];
    char* ws = (char*)d_ws;
    (void)hipFuncSetAttribute((const void*)k_attn, hipFuncAttributeMaxDynamicSharedMemorySize, 128 * 1024);
    k_prep<<<274, 256, 0, stream>>>(x, (const uint16_t*)d_in[1], (const uint16_t*)d_in[2], (const uint16_t*)d_in[3], (const uint16_t*)d_in[4],
                                    (const uint16_t*)d_in[6], (const uint16_t*)d_in[7], (const uint16_t*)d_in[8], ws);
    for (int l = 0; l < 2; ++l) {
        k_qkv<<<256, 512, 0, stream>>>(ws, 2 * l);
        k_attn<<<256, 512, 128 * 1024, stream>>>(ws);
        k_oproj<<<256, 256, 0, stream>>>(ws, 2 * l, 2 * l + 1);
        k_gemm1<<<256, 256, 0, stream>>>(ws, 2 * l + 1);
        k_gemm2<<<256, 256, 0, stream>>>(ws, 2 * l + 1, 2, l == 1 ? (uint16_t*)d_out : nullptr);
    }
}
```

```cpp
#include <hip/hip_runtime.h>
#include <hip/hip_bf16.h>
#include <stdint.h>
#include <type_traits>

#define FLIP 20
#define TOUCH 1
#define T45 8
#define T6 18
#define DIST 6
#define SAMEH 0
#define P3AT 4
#define PS0 19
#define PS1 2
#define RING 4
#define DSL 0
#define NOHST 0
#define CODEPF 1
typedef unsigned u32;
typedef u32 u32x2 __attribute__((ext_vector_type(2)));
typedef u32 u32x4 __attribute__((ext_vector_type(4)));
typedef float f32x2 __attribute__((ext_vector_type(2)));
typedef float f32x4 __attribute__((ext_vector_type(4)));
typedef float f32x8 __attribute__((ext_vector_type(8)));
typedef float f32x16 __attribute__((ext_vector_type(16)));
typedef __bf16 bf16x2 __attribute__((ext_vector_type(2)));
typedef __bf16 bf16x8 __attribute__((ext_vector_type(8)));
typedef __bf16 bf16x16 __attribute__((ext_vector_type(16)));
#define DI __device__ __forceinline__

constexpr int S_ = 2048, D_ = 448, F_ = 1792, DH = 28;
constexpr size_t KB = 1024;
constexpr size_t OFF_WQ = 0;
constexpr size_t OFF_WKV = OFF_WQ + 448 * KB;
constexpr size_t OFF_WO = OFF_WKV + 56 * KB;
constexpr size_t OFF_WGU = OFF_WO + 512 * KB;
constexpr size_t OFF_WD = OFF_WGU + 3136 * KB;
constexpr size_t OFF_CS = OFF_WD + 1792 * KB;
constexpr size_t OFF_CG = OFF_CS + 4 * KB;
constexpr size_t OFF_QF = OFF_CG + 56 * KB;
constexpr size_t OFF_KV = OFF_QF + 2048 * KB;
constexpr size_t OFF_KN = OFF_KV + 2 * 256 * KB;
constexpr size_t OFF_HO = OFF_KN + 4 * KB;
constexpr size_t OFF_X1 = OFF_HO;
constexpr size_t OFF_X2 = OFF_X1 + 1792 * KB;
constexpr size_t OFF_S1 = OFF_X2 + 1792 * KB;
constexpr size_t OFF_S2 = OFF_S1 + 256 * KB;
constexpr size_t OFF_OF = OFF_S2 + 256 * KB;
constexpr size_t OFF_HF = OFF_OF + 2048 * KB;
constexpr size_t WS_END = OFF_HF + 7168 * KB;
constexpr int HO_KB = (int)((WS_END - OFF_HO) / KB);

#define STAMP_AT(i)
#define STAMP_INIT()
#define STAMP_DUMP()
DI f32x16 mfma(u32x4 a, u32x4 b, f32x16 c) {
    return __builtin_amdgcn_mfma_f32_32x32x16_bf16(__builtin_bit_cast(bf16x8, a), __builtin_bit_cast(bf16x8, b), c, 0, 0, 0);
}
DI f32x16 smfmac_hi(u32x4 a, f32x8 b, f32x16 c) {
    return __builtin_amdgcn_smfmac_f32_32x32x32_bf16(__builtin_bit_cast(bf16x8, a), __builtin_bit_cast(bf16x16, b), c, 0xDDDD, 0, 0);
}
DI u32 pk(float lo, float hi) { f32x2 v = {lo, hi}; return __builtin_bit_cast(u32, __builtin_convertvector(v, bf16x2)); }
DI float dot2(u32 a, u32 b, float c) { return __builtin_amdgcn_fdot2_f32_bf16(__builtin_bit_cast(bf16x2, a), __builtin_bit_cast(bf16x2, b), c, false); }
DI float blo(u32 x) { return __uint_as_float(x << 16); }
DI float bhi(u32 x) { return __uint_as_float(x & 0xffff0000u); }
DI float xhalf_sum(float v) { auto r = __builtin_amdgcn_permlane32_swap(__float_as_uint(v), __float_as_uint(v), false, false); return __uint_as_float(r[0]) + __uint_as_float(r[1]); }
DI float xhalf_max(float v) { auto r = __builtin_amdgcn_permlane32_swap(__float_as_uint(v), __float_as_uint(v), false, false); return fmaxf(__uint_as_float(r[0]), __uint_as_float(r[1])); }
DI u32x4 pack8(const f32x16& x, int s) { return (u32x4){pk(x[8 * s], x[8 * s + 1]), pk(x[8 * s + 2], x[8 * s + 3]), pk(x[8 * s + 4], x[8 * s + 5]), pk(x[8 * s + 6], x[8 * s + 7])}; }
DI int prow(int h, int j) { return 8 * (j >> 2) + 4 * h + (j & 3); }
DI int feat(int ks, int h, int j) {
    const int p = ks / 7, s = ks % 7;
    if (s < 6) return 112 * p + (s >= 3 ? 56 + 16 * (s - 3) : 16 * s) + prow(h, j);
    return 112 * p + 56 * (j >> 2) + 48 + 4 * h + (j & 3);
}

DI void ln_stats(float s1, float s2, float& mean, float& rstd) {
    s1 = xhalf_sum(s1); s2 = xhalf_sum(s2);
    mean = s1 * (1.f / D_);
    const float var = fmaxf(s2 * (1.f / D_) - mean * mean, 0.f);
    rstd = __builtin_amdgcn_rsqf(var + 1e-5f);
}

typedef __amdgpu_buffer_rsrc_t rsrc_t;
typedef __attribute__((address_space(3))) char* lds_t;
constexpr u32 ARMED = 0xFFFFFFFFu;
DI u32x4 ld(rsrc_t r, u32 v, u32 s) { return __builtin_amdgcn_raw_buffer_load_b128(r, v, s, 0); }
DI u32x4 ldx(rsrc_t r, u32 v, u32 s) { return __builtin_amdgcn_raw_buffer_load_b128(r, v, s, 16); }
DI void st(u32x4 x, rsrc_t r, u32 v, u32 s) { __builtin_amdgcn_raw_buffer_store_b128(x, r, v, s, 0); }
DI void stx(u32x4 x, rsrc_t r, u32 v, u32 s) { __builtin_amdgcn_raw_buffer_store_b128(x, r, v, s, 16); }
DI void stx2(u32x2 x, rsrc_t r, u32 v, u32 s) { __builtin_amdgcn_raw_buffer_store_b64(x, r, v, s, 16); }
DI void dma(rsrc_t r, u32 v, u32 s, u32 l) { asm volatile("s_mov_b32 m0, %0\n\ts_nop 0\n\tbuffer_load_dwordx4 %1, %2, %3 offen lds" ::"s"(l), "v"(v), "s"(r), "s"(s) : "m0", "memory"); }
DI u32x4 lds_ld(lds_t p, u32 off) { return *(__attribute__((address_space(3))) u32x4*)(p + off); }
DI void lds_st(lds_t p, u32 off, u32x4 v) { *(__attribute__((address_space(3))) u32x4*)(p + off) = v; }
DI u32 umax(u32 a, u32 b) { return a > b ? a : b; }
DI u32 umin(u32 a, u32 b) { return a < b ? a : b; }
template <int MODE> DI u32 mark1(u32 a) { return MODE == 0 ? (a & ~1u) : MODE == 1 ? (a | 1u) : umin(a, ARMED - 1); }
template <int MODE> DI u32x4 mark(u32x4 a) { return (u32x4){mark1<MODE>(a[0]), mark1<MODE>(a[1]), mark1<MODE>(a[2]), mark1<MODE>(a[3])}; }
template <int MODE> DI u32 fold(u32 m, u32x4 a) {
    return MODE == 0 ? ((m | a[0] | a[1]) | a[2] | a[3]) : MODE == 1 ? ((m & a[0] & a[1]) & a[2] & a[3]) : umax(umax(umax(m, a[0]), a[1]), umax(a[2], a[3]));
}
template <int MODE, int N> DI u32 fold_all(const u32x4* x) {
    u32 m = MODE == 1 ? ~0u : 0u;
#pragma unroll
    for (int i = 0; i < N; ++i) m = fold<MODE>(m, x[i]);
    return m;
}
template <int MODE> DI bool all_written(u32 m) { return __all(MODE == 0 ? (m & 1u) == 0u : MODE == 1 ? (m & 1u) != 0u : m != ARMED); }
typedef unsigned long long u64;
template <int MODE> DI u64 unwritten(u32 m) { u64 b = __ballot(MODE == 0 ? (m & 1u) != 0u : MODE == 1 ? (m & 1u) == 0u : m == ARMED); asm volatile("" : "+s"(b)); return b; }
#define SB() __builtin_amdgcn_sched_barrier(0)
#define RAWBAR() asm volatile("s_barrier" ::: "memory")
#define PIN(x) asm volatile("" : "+v"(x))
#define BAR() asm volatile("s_waitcnt lgkmcnt(0)\n\ts_barrier" ::: "memory")
#define VM0() asm volatile("s_waitcnt vmcnt(0)" ::: "memory")
constexpr int SPIN_CAP = 1 << 15;
#define COUNT_RETRY()
struct Wait { bool dead = false; int retries = 0; };
template <int MODE, int N, class F> DI void settle(u32x4* x, u64 bad, Wait& wt, F&& reload) {
    if (__builtin_expect(bad != 0, 0)) {
        for (int sp = 0; !wt.dead; ++sp) {
            __builtin_amdgcn_s_sleep(1);
            asm volatile("" ::: "memory");
#pragma unroll
            for (int i = 0; i < N; ++i) x[i] = reload(i);
            if (all_written<MODE>(fold_all<MODE, N>(x))) break;
            if (sp > SPIN_CAP) wt.dead = true;
        }
    }
}
template <int MODE, class F> DI void gated(rsrc_t rs, u32 pv, u32 ps, Wait& wt, F&& batch) {
    for (int sp = 0;; ++sp) {
        asm volatile("" ::: "memory");
        const u32 p = __builtin_amdgcn_raw_buffer_load_b32(rs, pv, ps, 16);
        batch();
        asm volatile("" ::: "memory");
        if (__builtin_expect(all_written<MODE>(p) || wt.dead, 1)) break;
        if (sp > SPIN_CAP) wt.dead = true;
    }
}
DI u32 wave_umax(u32 v) {
    v = umax(v, __builtin_amdgcn_update_dpp(0u, v, 0x111, 0xf, 0xf, false));
    v = umax(v, __builtin_amdgcn_update_dpp(0u, v, 0x112, 0xf, 0xf, false));
    v = umax(v, __builtin_amdgcn_update_dpp(0u, v, 0x114, 0xf, 0xf, false));
    v = umax(v, __builtin_amdgcn_update_dpp(0u, v, 0x118, 0xf, 0xf, false));
    v = umax(v, __builtin_amdgcn_update_dpp(0u, v, 0x142, 0xa, 0xf, false));
    v = umax(v, __builtin_amdgcn_update_dpp(0u, v, 0x143, 0xc, 0xf, false));
    return __builtin_amdgcn_readlane(v, 63);
}

DI void load_cs(f32x4* c, int kind, rsrc_t rs, int tile, int lane) {
    if (kind != 2) {
#pragma unroll
        for (int i = 0; i < 4; ++i) c[i] = __builtin_bit_cast(f32x4, ld(rs, (lane >> 5) * 16, OFF_CS + tile * 128 + i * 32));
    } else c[0][0] = __uint_as_float(__builtin_amdgcn_raw_buffer_load_b32(rs, (lane & 31) * 4, OFF_CS + tile * 128, 0));
}
template <bool NORMED> DI void qkv_finish(f32x16 acc, int kind, float mean, float rstd, const f32x4* cs, rsrc_t rs, char* ws, u32 dst, int kt, float* sc, int lane) {
    const int m = lane & 31, h = lane >> 5;
    if (kind != 2) {
        const float s = kind == 0 ? rstd * (0.18898223650461362f * 1.4426950408889634f) : rstd;
#pragma unroll
        for (int i = 0; i < 4; ++i) { const f32x4 c = cs[i];
#pragma unroll
            for (int r = 0; r < 4; ++r) acc[4 * i + r] = NORMED ? ((i == 3 && h == 1) ? 0.f : acc[4 * i + r] * s) : (acc[4 * i + r] - mean * c[r]) * s; }
        u32x4 k0 = pack8(acc, 0), k1 = pack8(acc, 1);
        float nn = 0.f;
#pragma unroll
        for (int i = 0; i < 4; ++i) { nn = dot2(k0[i], k0[i], nn); nn = dot2(k1[i], k1[i], nn); }
        nn = xhalf_sum(nn);
        if (kind == 0) {
            if (h == 1) k1[2] = pk(__builtin_amdgcn_sqrtf(nn) * 1.01f, 0.f);
            st(k0, rs, lane * 16, dst); st(k1, rs, lane * 16, dst + 1024);
        } else {
            const u32 mx = wave_umax(__float_as_uint(nn));
            if (h == 1) k1[2] = 0x3f803f80u;
            const int slot = (m & 19) | ((m & 4) << 1) | ((m & 8) >> 1);
            st(k0, rs, (slot + 32 * h) * 16, dst); st(k1, rs, (slot + 32 * h) * 16, dst + 1024);
            if (lane == 0) ((u32*)(ws + OFF_KN))[kt] = mx;
        }
    } else if (NORMED) {
#pragma unroll
        for (int r = 0; r < 16; ++r) acc[r] = m < 28 ? acc[r] : (m == 28 ? 1.f : 0.f);
        st(pack8(acc, 0), rs, lane * 16, dst); st(pack8(acc, 1), rs, lane * 16, dst + 1024);
    } else {
        if (h == 0) { sc[m] = mean; sc[32 + m] = rstd; }
        __builtin_amdgcn_fence(__ATOMIC_RELEASE, "wavefront"); __builtin_amdgcn_wave_barrier(); __builtin_amdgcn_fence(__ATOMIC_ACQUIRE, "wavefront");
        const float cv = cs[0][0];
#pragma unroll
        for (int i = 0; i < 4; ++i) { const f32x4 mu = *(const f32x4*)(sc + 8 * i + 4 * h), rr = *(const f32x4*)(sc + 32 + 8 * i + 4 * h);
#pragma unroll
            for (int r = 0; r < 4; ++r) acc[4 * i + r] = m == 28 ? 1.f : (acc[4 * i + r] - mu[r] * cv) * rr[r]; }
        st(pack8(acc, 0), rs, lane * 16, dst); st(pack8(acc, 1), rs, lane * 16, dst + 1024);
    }
}

DI void stats_from(const u32x4* s, float& mean, float& rstd) {
    float s1 = 0.f, s2 = 0.f;
#pragma unroll
    for (int i = 0; i < 4; ++i) { s1 += __uint_as_float(s[i][0]) + __uint_as_float(s[i][2]); s2 += __uint_as_float(s[i][1]) + __uint_as_float(s[i][3]); }
    ln_stats(s1, s2, mean, rstd);
}

struct Sel { u32x4 a; u32 idx; };
DI Sel make_sel(int lane) {
    asm volatile("" : "+v"(lane));
    const int m = lane & 31, grp = ((m >> 1) & 2) | ((m >> 3) & 1);
    const u32 one = (m >> 4) == (lane >> 5) ? 0x3f80u : 0u;
    Sel s; s.idx = (m & 3) * 0x5555u;
#pragma unroll
    for (int i = 0; i < 4; ++i) s.a[i] = grp == i ? one : 0u;
    PIN(s.a); PIN(s.idx);
    return s;
}
DI f32x16 add_tile(const Sel& s, u32x4 lo, u32x4 hi, f32x16 c) {
    return __builtin_amdgcn_smfmac_f32_32x32x32_bf16(__builtin_bit_cast(bf16x8, s.a), __builtin_bit_cast(bf16x16, __builtin_shufflevector(lo, hi, 0, 1, 2, 3, 4, 5, 6, 7)), c, s.idx, 0, 0);
}
template <int N> DI f32x16 add_tiles(const Sel& s, lds_t lds, u32 off, u32 stride, f32x16 acc) {
    u32x4 t[N][2];
#pragma unroll
    for (int u = 0; u < N; ++u) { t[u][0] = lds_ld(lds, off + u * stride); t[u][1] = lds_ld(lds, off + u * stride + 1024); }
    SB();
#pragma unroll
    for (int u = 0; u < N; ++u) acc = add_tile(s, t[u][0], t[u][1], acc);
    return acc;
}

typedef short s16x4 __attribute__((ext_vector_type(4)));
DI void gdma(const void* g, u32 l) { asm volatile("s_mov_b32 m0, %0\n\ts_nop 0\n\tglobal_load_lds_dwordx4 %1, off" ::"s"(l), "v"(g) : "m0", "memory"); }
DI u32x4 tr8(lds_t lds, u32 pa, u32 pb) {
    const u32x2 lo = __builtin_bit_cast(u32x2, __builtin_amdgcn_ds_read_tr16_b64_v4i16((__attribute__((address_space(3))) s16x4*)(lds + pa)));
    const u32x2 hi = __builtin_bit_cast(u32x2, __builtin_amdgcn_ds_read_tr16_b64_v4i16((__attribute__((address_space(3))) s16x4*)(lds + pb)));
    return (u32x4){lo[0], lo[1], hi[0], hi[1]};
}
#define FAUX 16
#define WAUX 16
DI void stw(u32x4 x, rsrc_t r, u32 v, u32 s) { __builtin_amdgcn_raw_buffer_store_b128(x, r, v, s, WAUX); }
constexpr u32 F_WK = 50176, F_ST = 75264, F_SC = F_ST + 2048, F_TOTAL = 80 * 1024;
constexpr int N_FIRST = 256 + 112 + 56 + 16;
__global__ void __launch_bounds__(512, 4) k_first(const uint16_t* x, const uint16_t* wq, const uint16_t* wk, const uint16_t* wv, const uint16_t* wo,
                                                  const uint16_t* wg, const uint16_t* wu, const uint16_t* wd, char* ws) {
    extern __shared__ __attribute__((aligned(16))) char smem[];
    const lds_t lds = (lds_t)smem;
    const u32 lds0 = (u32)(uintptr_t)lds;
    const int b = blockIdx.x, w = __builtin_amdgcn_readfirstlane(threadIdx.x >> 6), lane = threadIdx.x & 63, m = lane & 31, h = lane >> 5;
    const u32 l16 = lane * 16;
    const rsrc_t rs = __builtin_amdgcn_make_buffer_rsrc(ws, 0, (int)WS_END, 0x00020000);
    const int q4 = (lane & 15) >> 2;
    const u32 c8 = ((lane >> 4) & 1) * 32 + (lane & 3) * 8;
    auto arm = [&]() {
        const u32x4 a4 = {ARMED, ARMED, ARMED, ARMED};
#pragma unroll
        for (int i = 0; i < 4; ++i) { const int e = b * 8 + w + i * (N_FIRST * 8); if (e < HO_KB) __builtin_amdgcn_raw_buffer_store_b128(a4, rs, l16, OFF_HO + e * 1024, FAUX); }
    };
    if (b < 256) {
        const int g = (b & 7) * 4 + (b >> 6), j8 = (b >> 3) & 7, nn = w & 1, kq = w >> 1;
        const bool kvwg = j8 < 4, kvjob = kvwg && nn == (j8 & 1), isv = j8 >> 1;
        u32x4 xf[7];
        { const uint16_t* xr = x + (size_t)(64 * g + 32 * nn + m) * D_ + 112 * kq + 8 * h;
#pragma unroll
          for (int i = 0; i < 7; ++i) xf[i] = *(const u32x4*)(xr + 16 * i); }
#pragma unroll
        for (int i = 0; i < 7; ++i) { const int p = w + 8 * i, c = p * 64 + lane; if (p < 49) gdma(wq + (size_t)(c / 7) * D_ + 56 * j8 + 8 * (c % 7), lds0 + p * 1024); }
        if (kvwg) {
#pragma unroll
            for (int i = 0; i < 4; ++i) { const int p = w + 8 * i, c = p * 64 + lane; if (p < 25 && c < 1568) gdma((isv ? wv : wk) + 8 * c, lds0 + F_WK + p * 1024); }
        }
        arm();
        float s1 = 0.f, s2 = 0.f;
#pragma unroll
        for (int i = 0; i < 7; ++i)
#pragma unroll
            for (int e = 0; e < 4; ++e) { s1 = dot2(xf[i][e], 0x3f803f80u, s1); s2 = dot2(xf[i][e], xf[i][e], s2); }
        *(f32x2*)(smem + F_ST + (nn * 4 + kq) * 256 + m * 8) = (f32x2){xhalf_sum(s1), xhalf_sum(s2)};
        asm volatile("s_waitcnt vmcnt(4)" ::: "memory");
        if (b * 8 + w + 3 * (N_FIRST * 8) >= HO_KB) asm volatile("s_waitcnt vmcnt(3)" ::: "memory");
        BAR();
        { s1 = 0.f; s2 = 0.f;
#pragma unroll
          for (int u = 0; u < 4; ++u) { const f32x2 t = *(const f32x2*)(smem + F_ST + (nn * 4 + u) * 256 + m * 8); s1 += t[0]; s2 += t[1]; }
          const float mean = s1 * (1.f / D_), rstd = __builtin_amdgcn_rsqf(fmaxf(s2 * (1.f / D_) - mean * mean, 0.f) + 1e-5f), nb = -mean * rstd;
#pragma unroll
          for (int i = 0; i < 7; ++i)
#pragma unroll
              for (int e = 0; e < 4; ++e) xf[i][e] = pk(fmaf(blo(xf[i][e]), rstd, nb), fmaf(bhi(xf[i][e]), rstd, nb)); }
        f32x16 qa[2] = {}, ka = {};
        const u32 rq = (112 * kq + 8 * h + q4) * 112 + c8, rk = F_WK + (112 * kq + 8 * h + q4) * 56 + c8;
#pragma unroll
        for (int i = 0; i < 7; ++i) {
            qa[0] = mfma(tr8(lds, rq + i * 1792, rq + i * 1792 + 448), xf[i], qa[0]);
            qa[1] = mfma(tr8(lds, rq + i * 1792 + 56, rq + i * 1792 + 504), xf[i], qa[1]);
        }
        if (kvjob) {
#pragma unroll
            for (int i = 0; i < 7; ++i) { const u32x4 a = tr8(lds, rk + i * 896, rk + i * 896 + 224); ka = isv ? mfma(xf[i], a, ka) : mfma(a, xf[i], ka); }
        }
        const bool emitq = g == 0, emitk = g == 1 && (j8 == 0 || j8 == 2) && w < 4;
        u32x4 ef[7];
        if (emitq || emitk) {
            const u32 pitch = emitq ? 112 : 56, base = (emitq ? (w >> 2) * 56 : F_WK) + c8;
#pragma unroll
            for (int i = 0; i < 7; ++i) { const int r0 = feat(7 * (w & 3) + i, h, 0) + q4, r1 = feat(7 * (w & 3) + i, h, 4) + q4; ef[i] = tr8(lds, base + r0 * pitch, base + r1 * pitch); }
        }
        const Sel sel = make_sel(lane);
#pragma unroll
        for (int r = 12; r < 16; ++r) { qa[0][r] = h ? 0.f : qa[0][r]; qa[1][r] = h ? 0.f : qa[1][r]; ka[r] = (h && !isv) ? 0.f : ka[r]; }
        BAR();
#pragma unroll
        for (int c = 0; c < 4; ++c) lds_st(lds, (kq * 10 + (c >> 1) * 4 + nn * 2 + (c & 1)) * 1024 + l16, pack8(qa[c >> 1], c & 1));
        if (kvjob) { lds_st(lds, (kq * 10 + 8) * 1024 + l16, pack8(ka, 0)); lds_st(lds, (kq * 10 + 9) * 1024 + l16, pack8(ka, 1)); }
        if (emitq || emitk) {
            const int tile = emitq ? 2 * j8 + (w >> 2) : 16 + (j8 >> 1);
            float cs = 0.f;
#pragma unroll
            for (int i = 0; i < 7; ++i) {
                if (m >= 28) ef[i] = (u32x4){0u, 0u, 0u, 0u};
                stw(ef[i], rs, l16, OFF_WQ + (tile * 28 + 7 * (w & 3) + i) * 1024);
#pragma unroll
                for (int e = 0; e < 4; ++e) cs = dot2(ef[i][e], 0x3f803f80u, cs);
            }
            *(float*)(smem + F_SC + w * 128 + m * 4) = xhalf_sum(cs);
        }
        BAR();
        if (w < 4 || (w == 4 && kvwg)) {
            const f32x16 y = add_tiles<4>(sel, lds, 2 * w * 1024 + l16, 10240, f32x16{});
            const int sn = 2 * g + (w < 4 ? nn : (j8 & 1));
            const u32 dst = w < 4 ? (u32)OFF_QF + ((g * 8 + j8) * 4 + w) * 2048 : (u32)OFF_KV + (sn * 4 + (isv ? 2 : 0)) * 1024;
            qkv_finish<true>(y, w < 4 ? 0 : (isv ? 2 : 1), 0.f, 1.f, nullptr, rs, ws, dst, sn, nullptr, lane);
        } else if (w == 7 && (emitq || (g == 1 && (j8 == 0 || j8 == 2)))) {
            const float* p = (const float*)(smem + F_SC) + h * 128 + m;
            if (emitq || h == 0) ((float*)(ws + OFF_CS))[(emitq ? 2 * j8 + h : 16 + (j8 >> 1)) * 32 + m] = (p[0] + p[32]) + (p[64] + p[96]);
        }
    } else if (b < 256 + 112) {
        const int jb = b - 256, tq = jb >> 2, p4 = jb & 3;
#pragma unroll
        for (int i = 0; i < 4; ++i) { const int p = w + 8 * i;
            if (p < 28) gdma(((lane & 8) ? wu : wg) + (size_t)(112 * p4 + 4 * p + (lane >> 4)) * F_ + 64 * tq + 8 * (lane & 7), lds0 + p * 1024); }
        arm();
        asm volatile("s_waitcnt vmcnt(3)" ::: "memory");
        BAR();
        const int tt = w >> 1, s0 = (w & 1) * 4;
        const u32 base = ((lane >> 4) & 1) * 128 + tt * 32 + (lane & 3) * 8;
        float cs = 0.f;
#pragma unroll
        for (int i = 0; i < 4; ++i) if (s0 + i < 7) {
            const u32x4 f = tr8(lds, base + (feat(s0 + i, h, 0) + q4) * 256, base + (feat(s0 + i, h, 4) + q4) * 256);
            stw(f, rs, l16, OFF_WGU + ((4 * tq + tt) * 28 + 7 * p4 + s0 + i) * 1024);
#pragma unroll
            for (int e = 0; e < 4; ++e) cs = dot2(f[e], 0x3f803f80u, cs);
        }
        *(float*)(smem + 32768 + w * 128 + m * 4) = xhalf_sum(cs);
        BAR();
        if (threadIdx.x < 128) { const float* p = (const float*)(smem + 32768) + (threadIdx.x >> 5) * 64 + m;
            ((float*)(ws + OFF_CG))[(p4 * 112 + 4 * tq) * 32 + threadIdx.x] = p[0] + p[32]; }
    } else {
        const bool isd = b < 256 + 112 + 56;
        const int jb = isd ? b - 368 : b - 424, nch = isd ? 1792 : 1568;
        const uint16_t* src = isd ? wd + (size_t)jb * 32 * D_ : wo + (size_t)jb * 28 * D_;
#pragma unroll
        for (int i = 0; i < 4; ++i) { const int p = w + 8 * i, c = p * 64 + lane; if (p < 28 && c < nch) gdma(src + 8 * c, lds0 + p * 1024); }
        arm();
        asm volatile("s_waitcnt vmcnt(3)" ::: "memory");
        BAR();
#pragma unroll
        for (int i = 0; i < 4; ++i) {
            const int mt = i >> 1, s = i & 1;
            const u32 a0 = (16 * s + 4 * h + q4) * 896 + 112 * w + 64 * mt + c8;
            u32x4 f = tr8(lds, a0, a0 + 8 * 896);
            if (mt == 1 && m >= 24) f = (u32x4){0u, 0u, 0u, 0u};
            if (!isd && s == 1 && h == 1) { f[2] = 0u; f[3] = 0u; }
            stw(f, rs, l16, isd ? OFF_WD + ((w * 2 + mt) * 112 + 2 * jb + s) * 1024 : OFF_WO + ((w * 2 + mt) * 32 + 2 * jb + s) * 1024);
        }
    }
}

constexpr u32 L_RED = 0, L_GU0 = 64 * 1024, L_GU3 = 32 * 1024, L_GU4 = 0, L_GU5 = 64 * 1024, L_GU6 = 92 * 1024, L_WD1 = 120 * 1024, L_WD2 = 0,
              L_CS = 152 * 1024, L_SC = 154 * 1024, L_WQ = 0, L_WKV = 56 * 1024, L_PE = 84 * 1024, L_TOTAL = 160 * 1024;

template <int lay> __global__ void __launch_bounds__(512) k_layer(char* ws, const uint16_t* xin, uint16_t* out) {
    extern __shared__ __attribute__((aligned(16))) char smem[];
    const lds_t lds = (lds_t)smem;
    const u32 lds0 = (u32)(uintptr_t)lds;
    const int b = blockIdx.x, g = (b & 7) * 4 + (b >> 6), j8 = (b >> 3) & 7;
    const int blk1 = (b & 1) * 8 + ((b >> 3) & 7), i16 = ((b >> 1) & 3) * 4 + (b >> 6);
    const int w = __builtin_amdgcn_readfirstlane(threadIdx.x >> 6);
    const u32 l16 = (threadIdx.x & 63) * 16;
#define FRESH_LANE() u32 l16_ = l16; PIN(l16_); const int lane = l16_ >> 4, m = lane & 31, h = lane >> 5; (void)m; (void)h
    const rsrc_t rs = __builtin_amdgcn_make_buffer_rsrc(ws, 0, (int)WS_END, 0x00020000);
    constexpr u32 o_x1 = OFF_X1, o_x2 = OFF_X2, o_st1 = OFF_S1, o_st2 = OFF_S2, o_of = OFF_OF, o_hf = OFF_HF;
    const int mt = (w >> 1) & 1, nn = w & 1, ntile = 2 * g + nn;
    const int ks0 = 7 * (j8 >> 1) + 3 * (j8 & 1), ksh = 7 * (j8 >> 1) + 6;
    const u32 xo0 = (ntile * 28 + ks0 + 2 * mt) * 1024, xo1 = mt ? (ntile * 28 + ksh) * 1024 + 8 * (j8 & 1) : xo0 + 1024;
    const u32 sto = ntile * 4096 + j8 * 512 + mt * 8;
    Wait wt;
    u64 pc0; asm volatile("s_getpc_b64 %0" : "=s"(pc0));
    STAMP_INIT();
    STAMP_AT(0 + 16 * lay);

    const u32 po = w < 4 ? OFF_WO + (j8 * 64 + 8 * w) * 1024 : OFF_WD + (j8 * 224 + 28 * (w - 4)) * 1024;
    const u32 l16o = l16;
    auto attend = [&](auto role, u32x4* P) {
        constexpr bool A = decltype(role)::value;
        u32 l16 = l16o;
        if (A) asm volatile("; waves 0-3" : "+v"(l16)); else asm volatile("; waves 4-7" : "+v"(l16));
    const u32 kvo = OFF_KV + (lay * 256 + w * 32) * 1024;
    if (!A) __builtin_amdgcn_s_setprio(1);
    u32x4 q[4][2], kf[2][4];
    dma(rs, l16, OFF_QF + ((g * 8 + j8) * 8 + w) * 1024, lds0 + w * 1024);
    const u32 knv = __builtin_amdgcn_raw_buffer_load_b32(rs, l16 >> 2, OFF_KN + lay * 256, 0);
#pragma unroll
    for (int i = 0; i < 4; ++i) kf[0][i] = ld(rs, l16, kvo + i * 1024);
    kf[1][0] = ld(rs, l16, kvo + 4096);
    u32x4 code = {};
    if (CODEPF) code = *(const __attribute__((address_space(1))) u32x4*)((pc0 & ~15ull) + (((b >> 3) * 8 + w) % 36) * 1024 + l16);
    if (CODEPF) asm volatile("s_waitcnt vmcnt(7)\n\ts_barrier" ::: "memory"); else asm volatile("s_waitcnt vmcnt(6)\n\ts_barrier" ::: "memory");
#pragma unroll
    for (int i = 0; i < 8; ++i) q[i >> 1][i & 1] = lds_ld(lds, i * 1024 + l16);
    {
        FRESH_LANE();
        const float kn = __builtin_amdgcn_sqrtf(__uint_as_float(wave_umax(knv))) * 1.01f;
        float sh[4]; bool safe = true;
#pragma unroll
        for (int t = 0; t < 4; ++t) { sh[t] = blo(q[t][1][2]) * kn; safe = safe && (h == 0 || sh[t] <= 48.f); }
        if (__builtin_expect(!__all(safe), 0)) {
            float* mx = (float*)(smem + 8192);
            float rm[4] = {-INFINITY, -INFINITY, -INFINITY, -INFINITY};
#pragma unroll
            for (int t = 0; t < 4; ++t) if (h == 1) q[t][1][2] = 0u;
#pragma unroll 1
            for (int kt = 0; kt < 8; ++kt) {
                const u32x4 k0 = ld(rs, l16, kvo + kt * 4096), k1 = ld(rs, l16, kvo + kt * 4096 + 1024);
#pragma unroll
                for (int t = 0; t < 4; ++t) {
                    f32x16 s = {}; s = mfma(k0, q[t][0], s); s = mfma(k1, q[t][1], s);
#pragma unroll
                    for (int r = 0; r < 16; ++r) rm[t] = fmaxf(rm[t], s[r]);
                }
            }
#pragma unroll
            for (int t = 0; t < 4; ++t) { rm[t] = xhalf_max(rm[t]); if (h == 0) mx[(w * 4 + t) * 32 + m] = rm[t]; }
            __syncthreads();
#pragma unroll
            for (int t = 0; t < 4; ++t) { float v = mx[t * 32 + m];
#pragma unroll
                for (int u = 1; u < 8; ++u) v = fmaxf(v, mx[(u * 4 + t) * 32 + m]);
                sh[t] = v; }
            __syncthreads();
        }
#pragma unroll
        for (int t = 0; t < 4; ++t) {
            const u32 hi = pk(-sh[t], 0.f) & 0xffffu; const u32 lo = pk(-sh[t] - blo(hi), 0.f);
            if (h == 1) q[t][1][2] = hi | (lo << 16);
        }
    }
    if (CODEPF) PIN(code);
    STAMP_AT(1 + 16 * lay);
        f32x16 o[4] = {};
        f32x16 sb[3];
        const f32x16 zero = {};
        sb[0] = mfma(kf[0][0], q[0][0], zero); sb[0] = mfma(kf[0][1], q[0][1], sb[0]);
#define EX1(x) do { float t_ = __builtin_amdgcn_exp2f(x); PIN(t_); x = t_; } while (0)
#define EX4(S, i) do { EX1(S[i]); EX1(S[i + 1]); EX1(S[i + 2]); EX1(S[i + 3]); SB(); } while (0)
        SB();
#pragma unroll
        for (int u = 0; u < 32; ++u) {
            const int kt = u >> 2, t = u & 3, un = u + 1, ktn = un >> 2, tn = un & 3, up = u - 1;
            f32x16& cur = sb[u % 3]; f32x16& nxt = sb[un % 3]; f32x16& prv = sb[(u + 2) % 3];
            if (un < 32) nxt = mfma(kf[ktn & 1][0], q[tn][0], zero);
            SB();
            if (u < 6 && A) {
#pragma unroll
                for (int i = 3 * u; i < 3 * u + 3 && i < 14; ++i) P[i] = ld(rs, l16, po + ((i >> 3) * 32 + (i & 7)) * 1024);
            }
            SB(); EX4(cur, 0);
            if (un < 32) nxt = mfma(kf[ktn & 1][1], q[tn][1], nxt);
            SB(); EX4(cur, 4);
            if (up >= 0) o[up & 3] = smfmac_hi(kf[(up >> 2) & 1][3], __builtin_shufflevector(prv, prv, 8, 9, 10, 11, 12, 13, 14, 15), o[up & 3]);
            SB();
            if (t == 3) { if (kt + 2 < 8) kf[kt & 1][0] = ld(rs, l16, kvo + (kt + 2) * 4096); }
            else if (kt + 1 < 8) kf[(kt + 1) & 1][t + 1] = ld(rs, l16, kvo + ((kt + 1) * 4 + t + 1) * 1024);
            SB(); EX4(cur, 8);
            o[t] = smfmac_hi(kf[kt & 1][2], __builtin_shufflevector(cur, cur, 0, 1, 2, 3, 4, 5, 6, 7), o[t]);
            SB();
            if (u < 7 && A) {
#pragma unroll
                for (int p = 3 * u; p < 3 * u + 3; ++p) dma(rs, l16, OFF_WGU + (i16 * 196 + w * 21 + p) * 1024, lds0 + L_GU0 + (w * 21 + p) * 1024);
            }
            if (u >= 26 && !A) {
#pragma unroll
                for (int i = 3 * (u - 26); i < 3 * (u - 26) + 3 && i < 16; ++i) P[i] = ld(rs, l16, po + ((i & 1) * 112 + 7 * (((i >> 1) + j8) & 3) + (i >> 3)) * 1024);
            }
            if (u == FLIP && !A) __builtin_amdgcn_s_setprio(0);
            SB(); EX4(cur, 12);
        }
        o[3] = smfmac_hi(kf[1][3], __builtin_shufflevector(sb[31 % 3], sb[31 % 3], 8, 9, 10, 11, 12, 13, 14, 15), o[3]);
        STAMP_AT(2 + 16 * lay);
#pragma unroll
        for (int c = 0; c < 8; ++c) lds_st(lds, L_RED + (w * 8 + c) * 1024 + l16, pack8(o[c >> 1], c & 1));
    };
    if (w < 4) {
        u32x4 P[16], res[2];
        attend(std::true_type{}, P);
        SB();
        P[14] = ld(rs, l16, po + 38 * 1024); P[15] = ld(rs, l16, po + 39 * 1024);
        if (lay) { res[0] = ld(rs, l16, o_x2 + xo0); res[1] = ld(rs, l16, o_x2 + (xo1 & ~15u)); }
        else {
            FRESH_LANE();
            const uint16_t* r = xin + (size_t)(32 * ntile + m) * D_ + 56 * j8 + 32 * mt + 4 * h;
            const u32x2 r0 = *(const u32x2*)r, r1 = *(const u32x2*)(r + 8), r2 = *(const u32x2*)(r + 16), r3 = *(const u32x2*)(r + (mt ? 16 : 24));
            res[0] = (u32x4){r0[0], r0[1], r1[0], r1[1]}; res[1] = (u32x4){r2[0], r2[1], r3[0], r3[1]};
        }
        SB();
        const Sel sel = make_sel(l16 >> 4);
        f32x16 acc0 = {}; PIN(acc0);
        BAR();
        STAMP_AT(3 + 16 * lay);
        {
            f32x16 acc = add_tiles<8>(sel, lds, L_RED + 2 * w * 1024 + l16, 8192, acc0);
            BAR();
            const auto sw = __builtin_amdgcn_permlane32_swap(__float_as_uint(acc[12]), __float_as_uint(acc[12]), false, false);
            const float il = __builtin_amdgcn_rcpf(__uint_as_float(sw[1]));
#pragma unroll
            for (int r = 0; r < 16; ++r) acc[r] *= il;
            const u32 oo = o_of + (ntile * 32 + 4 * j8 + 2 * mt) * 1024;
            stx(mark<lay>(pack8(acc, 0)), rs, l16, oo); stx(mark<lay>(pack8(acc, 1)), rs, l16, oo + 1024);
        }
        STAMP_AT(4 + 16 * lay);
        VM0();
        STAMP_AT(5 + 16 * lay);
        u32x4 xr[2];
        {
            u32x4 ob[16];
            const u32 oo = o_of + (2 * g * 32 + 8 * w) * 1024;
            gated<lay>(rs, (((l16 >> 4) & 1) * 32 + ((l16 >> 5) & 3) * 2 + 1) * 1024, oo, wt, [&]() {
#pragma unroll
                for (int i = 0; i < 10; ++i) ob[i] = ldx(rs, l16, oo + ((i & 1) * 32 + (i >> 1)) * 1024); });
#pragma unroll
            for (int i = 10; i < 16; ++i) ob[i] = ldx(rs, l16, oo + ((i & 1) * 32 + (i >> 1)) * 1024);
            if (mt) res[1] = (lay && (j8 & 1)) ? (u32x4){res[1][2], res[1][3], 0u, 0u} : (u32x4){res[1][0], res[1][1], 0u, 0u};
            f32x16 y0 = add_tile(sel, res[0], res[1], f32x16{}); PIN(y0);
            f32x16 acc[4] = {};
#pragma unroll
            for (int c = 0; c < 4; ++c) {
                settle<lay, 4>(ob + 4 * c, unwritten<lay>(fold_all<lay, 4>(ob + 4 * c)), wt, [&](int i) { return ldx(rs, l16, oo + ((i & 1) * 32 + 2 * c + (i >> 1)) * 1024); });
#pragma unroll
                for (int i = 0; i < 4; ++i) { const int ks = 2 * c + (i >> 1), n = i & 1;
                    acc[n] = mfma(P[ks], ob[4 * c + i], acc[n]); acc[2 + n] = mfma(P[8 + ks], ob[4 * c + i], acc[2 + n]); }
            }
            STAMP_AT(6 + 16 * lay);
#pragma unroll
            for (int c = 0; c < 8; ++c) lds_st(lds, (w * 8 + c) * 1024 + l16, pack8(acc[c >> 1], c & 1));
            BAR();
            f32x16 y = add_tiles<4>(sel, lds, 2 * w * 1024 + l16, 8192, y0);
            BAR();
            xr[0] = pack8(y, 0); xr[1] = pack8(y, 1);
            stx(mark<lay>(xr[0]), rs, l16, o_x1 + xo0);
            if (mt) stx2((u32x2){mark1<lay>(xr[1][0]), mark1<lay>(xr[1][1])}, rs, l16, o_x1 + xo1); else stx(mark<lay>(xr[1]), rs, l16, o_x1 + xo1);
            float s1 = 0.f, s2 = 0.f;
#pragma unroll
            for (int r = 0; r < 16; ++r) { s1 += y[r]; s2 = fmaf(y[r], y[r], s2); }
            s1 = xhalf_sum(s1); s2 = xhalf_sum(s2);
            if (l16 < 512) stx2((u32x2){mark1<lay>(__float_as_uint(s1)), mark1<lay>(__float_as_uint(s2))}, rs, l16, o_st1 + sto);
        }
        STAMP_AT(7 + 16 * lay);
        {
            __builtin_amdgcn_s_setprio(2);
            FRESH_LANE();
            const int nt = 4 * blk1 + w;
            const u32 xo = o_x1 + nt * 28 * 1024, so = o_st1 + nt * 4096 + 0u, sv = (4 * h) * 512 + m * 16;
            u32x4 x[28], sr[4];
            f32x16 a[4]; const f32x16 zero = {};
            u32x4 ar[6], br[RING];
            u32 lb = l16 + L_GU0, lc = l16 + L_GU0 + 56 * 1024, lcs = L_CS + h * 16; PIN(lb); PIN(lc); PIN(lcs);
            auto gu012 = [&](int tt, int ks) { return tt < 2 ? lds_ld(lds, lb + (tt * 28 + ks) * 1024) : lds_ld(lds, lc + ks * 1024); };
            auto gu36 = [&](int e) { return e < 28 ? lds_ld(lds, l16 + L_GU3 + e * 1024) : e < 56 ? lds_ld(lds, l16 + L_GU4 + (e - 28) * 1024) : lds_ld(lds, lb + (e - 56) * 1024); };
#pragma unroll
            for (int i = 0; i < 6; ++i) ar[i] = gu012(i % 3, i / 3);
            const int pj = lane & 7;
            u32 pv = (lane & 8) ? (7 * (pj >> 1) + 6) * 1024 + 8 * (pj & 1) : (7 * (pj >> 1) + 3 * (pj & 1) + 1) * 1024; PIN(pv);
            SB();
            asm volatile("s_waitcnt vmcnt(1)" ::: "memory");
            STAMP_AT(8 + 16 * lay);
            gated<lay>(rs, pv, xo, wt, [&]() {
#pragma unroll
                for (int ks = 0; ks < 10; ++ks) x[ks] = ldx(rs, l16 + (ks & 3) * 1024, xo + (ks >> 2) * 4096); });
            settle<lay, 2>(x, unwritten<lay>(fold_all<lay, 2>(x)), wt, [&](int i) { return ldx(rs, l16, xo + i * 1024); });
            x[10] = ldx(rs, l16 + 2048, xo + 8192); x[11] = ldx(rs, l16 + 3072, xo + 8192);
            float nmean = 0.f;
            SB();
#pragma unroll
            for (int c = 0; c < 14; ++c) {
                u32 mx = 0; u64 bad = 0;
#pragma unroll
                for (int i = 0; i < 6; ++i) {
                    const int ks = 2 * c + i / 3, tt = i % 3, e = ks * 3 + tt;
                    a[tt] = mfma(ar[e % 6], x[ks], ks == 0 ? zero : a[tt]);
                    if (ks + 2 < 28) ar[e % 6] = gu012(tt, ks + 2); else if (i < RING) br[i] = gu36(i);
                    if (tt == 0) { if (ks + 12 < 28) x[ks + 12] = ldx(rs, l16 + (ks & 3) * 1024, xo + ((ks + 12) >> 2) * 4096); else if (ks < 20) sr[ks - 16] = ldx(rs, sv, so + (ks - 16) * 512); }
                    if (c < 13) {
                        if (i == 3) { mx = fold_all<lay, 2>(x + 2 * c + 2); PIN(mx); }
                        if (i == 4) { if (c == 12) { mx = fold<lay>(fold<lay>(mx, sr[0]), sr[1]); PIN(mx); } else bad = unwritten<lay>(mx); }
                        if (i == 5 && c == 12) bad = unwritten<lay>(fold<lay>(fold<lay>(mx, sr[2]), sr[3]));
                    } else {
                        if (i == 0) { nmean = (__uint_as_float(sr[0][0]) + __uint_as_float(sr[0][2])) + (__uint_as_float(sr[1][0]) + __uint_as_float(sr[1][2])); PIN(nmean); }
                        if (i == 1) { nmean += (__uint_as_float(sr[2][0]) + __uint_as_float(sr[2][2])) + (__uint_as_float(sr[3][0]) + __uint_as_float(sr[3][2])); PIN(nmean); }
                        if (i == 2) { nmean = xhalf_sum(nmean) * (-1.f / D_); PIN(nmean); }
                    }
                    SB();
                }
                if (c < 12) settle<lay, 2>(x + 2 * c + 2, bad, wt, [&](int i) { return ldx(rs, l16, xo + (2 * c + 2 + i) * 1024); });
                if (c == 12 && __builtin_expect(bad != 0, 0)) {
                    settle<lay, 2>(x + 26, 1, wt, [&](int i) { return ldx(rs, l16, xo + (26 + i) * 1024); });
                    settle<lay, 4>(sr, 1, wt, [&](int i) { return ldx(rs, sv, so + i * 512); });
                }
                SB();
            }
            STAMP_AT(9 + 16 * lay);
            f32x4 cs[2][4]; float hv[2][8]; u32x4 hp[2];
            auto epi = [&](int z, int e, int k, f32x16& v) {
                if (k < 2) { cs[z][2 * k] = __builtin_bit_cast(f32x4, lds_ld(lds, lcs + e * 128 + (2 * k) * 32)); cs[z][2 * k + 1] = __builtin_bit_cast(f32x4, lds_ld(lds, lcs + e * 128 + (2 * k + 1) * 32)); }
                else if (k >= 3 && k < 11) { const int r = k - 3;
                    float gg = fmaf(nmean, cs[z][r >> 2][r & 3], v[r]), uu = v[r + 8];
                    asm volatile("" : "+v"(gg), "+v"(uu));
                    hv[z][r] = gg * fmaf(nmean, cs[z][2 + (r >> 2)][r & 3], uu); PIN(hv[z][r]); }
                else if (k == 11) { hp[z][0] = mark1<lay>(pk(hv[z][0], hv[z][1])); hp[z][1] = mark1<lay>(pk(hv[z][2], hv[z][3])); PIN(hp[z]); }
                else if (k == 12) { hp[z][2] = mark1<lay>(pk(hv[z][4], hv[z][5])); hp[z][3] = mark1<lay>(pk(hv[z][6], hv[z][7])); PIN(hp[z]); }
                else if (k == 13 && (!NOHST || e == 6)) stx(hp[z], rs, l16, o_hf + (nt * 112 + 7 * i16 + e) * 1024);
            };
#pragma unroll
            for (int tt = 3; tt < 7; ++tt) {
#pragma unroll
                for (int ks = 0; ks < 28; ++ks) {
                    const int e = (tt - 3) * 28 + ks;
                    a[tt & 3] = mfma(br[e % RING], x[ks], ks == 0 ? zero : a[tt & 3]);
                    if (e == 2) RAWBAR();
                    if (e == 56 - RING) RAWBAR();
                    if (e == 68) RAWBAR();
                    if (e + RING < 112) br[e % RING] = gu36(e + RING);
                    if (tt == 3) epi(ks / 14, ks / 14, ks % 14, a[ks / 14]);
                    else if (tt == 4) epi(ks / 14, 2 + ks / 14, ks % 14, a[2 + ks / 14]);
                    else if (ks >= 1 && ks < 15) epi(0, tt - 1, ks - 1, a[(tt - 1) & 3]);
                    else if (tt == 6 && ks >= 26) epi(0, 6, ks - 26, a[2]);
                    SB();
                }
            }
#pragma unroll
            for (int k = 2; k < 14; ++k) epi(0, 6, k, a[2]);
            __builtin_amdgcn_s_setprio(0);
        }
        STAMP_AT(10 + 16 * lay);
        const Sel sel2 = make_sel(l16 >> 4);
        float r2; f32x16 y0 = {}, xres;
        { FRESH_LANE();
          const u32 so = o_st1 + ntile * 4096, sv = (4 * h) * 512 + m * 16;
          u32x4 sr[4];
#pragma unroll
          for (int i = 0; i < 4; ++i) sr[i] = ldx(rs, sv, so + i * 512);
          settle<lay, 4>(sr, unwritten<lay>(fold_all<lay, 4>(sr)), wt, [&](int i) { return ldx(rs, sv, so + i * 512); });
          float mean, rstd; stats_from(sr, mean, rstd); r2 = rstd * rstd;
#pragma unroll
          for (int i = 0; i < 8; ++i) { xres[2 * i] = blo(xr[i >> 2][i & 3]); xres[2 * i + 1] = bhi(xr[i >> 2][i & 3]); } }
        u32 lpd = L_WD1 + 2 * w * 1024 + l16; PIN(lpd); PIN(r2); PIN(y0); PIN(xres);
        BAR();
        STAMP_AT(11 + 16 * lay);
        {
            f32x16 y = add_tiles<4>(sel2, lds, lpd, 8192, y0);
#pragma unroll
            for (int r = 0; r < 16; ++r) y[r] = fmaf(y[r], r2, xres[r]);
            if (lay == 1) {
                FRESH_LANE();
                const u32x4 p0 = pack8(y, 0), p1 = pack8(y, 1);
                uint16_t* r = out + (size_t)(32 * ntile + m) * D_ + 56 * j8 + 32 * mt + 4 * h;
                *(u32x2*)r = (u32x2){p0[0], p0[1]}; *(u32x2*)(r + 8) = (u32x2){p0[2], p0[3]}; *(u32x2*)(r + 16) = (u32x2){p1[0], p1[1]};
                if (mt == 0) *(u32x2*)(r + 24) = (u32x2){p1[2], p1[3]};
            } else {
            const u32x4 p0 = mark<2>(pack8(y, 0)), p1 = mark<2>(pack8(y, 1));
            stx(p0, rs, l16, o_x2 + xo0);
            if (mt) stx2((u32x2){p1[0], p1[1]}, rs, l16, o_x2 + xo1); else stx(p1, rs, l16, o_x2 + xo1);
            float s1 = 0.f, s2 = 0.f;
#pragma unroll
            for (int r = 0; r < 16; ++r) { s1 += y[r]; s2 = fmaf(y[r], y[r], s2); }
            s1 = xhalf_sum(s1); s2 = xhalf_sum(s2);
            if (l16 < 512) stx2((u32x2){mark1<2>(__float_as_uint(s1)), mark1<2>(__float_as_uint(s2))}, rs, l16, o_st2 + sto);
            }
        }
        STAMP_AT(12 + 16 * lay);
        if (lay == 0) asm volatile("s_waitcnt vmcnt(1)" ::: "memory");
    } else {
        u32x4 P[16], P2[16];
        attend(std::false_type{}, P);
        const int kq = w - 4;
        const u32 gu = OFF_WGU + (i16 * 196 + kq * 7) * 1024, wdo = OFF_WD + (j8 * 224 + 28 * kq) * 1024;
        auto wdf = [&](int i, int t0) { return ld(rs, l16, wdo + ((i & 1) * 112 + 7 * (((i >> 1) + j8) & 3) + t0 + (i >> 3)) * 1024); };
        BAR();
        STAMP_AT(3 + 16 * lay);
#pragma unroll
        for (int i = 0; i < 8; ++i) P2[i] = wdf(i, 2);
        BAR();
#pragma unroll
        for (int i = 0; i < 7; ++i) dma(rs, l16, gu + (84 + i) * 1024, lds0 + L_GU3 + (kq * 7 + i) * 1024);
        { float cs = 0.f;
#pragma unroll
          for (int p = 0; p < 4; ++p) cs += __uint_as_float(__builtin_amdgcn_raw_buffer_load_b32(rs, l16 >> 2, OFF_CG + (p * 112 + 7 * i16) * 128 + kq * 224, 0));
          if (l16 < 56 * 16) *(float*)(smem + L_CS + kq * 224 + (l16 >> 2)) = cs; }
        VM0();
        BAR();
        u32x4 P3[16];
#pragma unroll
        for (int i = 8; i < 16; ++i) P2[i] = wdf(i, 2);
        BAR();
#pragma unroll
        for (int i = 0; i < 7; ++i) dma(rs, l16, gu + (112 + i) * 1024, lds0 + L_GU4 + (kq * 7 + i) * 1024);
#pragma unroll
        for (int i = 0; i < 4; ++i) P3[i] = wdf(i, 5);
        __builtin_amdgcn_s_sleep(PS0);
#pragma unroll
        for (int i = 4; i < 16; ++i) { P3[i] = wdf(i, 5); SB(); __builtin_amdgcn_s_sleep(PS1); SB(); }
        asm volatile("s_waitcnt vmcnt(16)" ::: "memory");
        RAWBAR();
#pragma unroll
        for (int i = 0; i < 7; ++i) { dma(rs, l16, gu + (140 + i) * 1024, lds0 + L_GU5 + (kq * 7 + i) * 1024); if (DSL) __builtin_amdgcn_s_sleep(DSL); }
#pragma unroll
        for (int i = 0; i < 7; ++i) { dma(rs, l16, gu + (168 + i) * 1024, lds0 + L_GU6 + (kq * 7 + i) * 1024); if (DSL) __builtin_amdgcn_s_sleep(DSL); }
        asm volatile("s_waitcnt vmcnt(7)" ::: "memory");
        RAWBAR();
#pragma unroll
        for (int i = 0; i < 8; ++i) { dma(rs, l16, wdo + ((i & 1) * 112 + 7 * (((i >> 1) + j8) & 3) + 4) * 1024, lds0 + L_WD1 + (kq * 8 + i) * 1024); if (DSL) __builtin_amdgcn_s_sleep(DSL); }
        asm volatile("s_waitcnt vmcnt(8)" ::: "memory");
        RAWBAR();
        STAMP_AT(8 + 16 * lay);
        {
            const u32 ho = o_hf + (2 * g * 112 + 28 * kq) * 1024;
            u32 hs[4][2], l16b = l16 + 4096; PIN(l16b);
#pragma unroll
            for (int i = 0; i < 8; ++i) { hs[i >> 1][i & 1] = ho + ((i & 1) * 112 + 7 * (((i >> 1) + j8) & 3)) * 1024; asm volatile("" : "+s"(hs[i >> 1][i & 1])); }
            auto hld = [&](int s, int n) { return SAMEH ? ldx(rs, l16, hs[s & 3][n]) : ldx(rs, (s < 16 ? l16 : l16b) + ((s >> 2) & 3) * 1024, hs[s & 3][n]); };
            u32x4 hb[28][2], al[2][2];
            auto window = [&](int s0, int s1, u64 bad) {
                if (__builtin_expect(bad != 0, 0)) {
                    for (int sp = 0; !wt.dead; ++sp) {
                        asm volatile("" ::: "memory");
#pragma unroll
                        for (int z = s0; z < s1 && z < 28; ++z) { hb[z][0] = hld(z, 0); hb[z][1] = hld(z, 1); }
                        __builtin_amdgcn_s_waitcnt(0x0F70);
                        if (all_written<lay>(fold_all<lay, 2>(hb[s0]))) break;
                        if (sp > SPIN_CAP) wt.dead = true;
                    }
                }
            };
#pragma unroll
            for (int s = 0; s < 5; ++s) { hb[s][0] = hld(s, 0); hb[s][1] = hld(s, 1); }
            SB();
#pragma unroll
            for (int i = 0; i < 16; ++i) lds_st(lds, L_WD2 + (kq * 16 + i) * 1024 + l16, P3[i]);
            SB();
            hb[5][0] = hld(5, 0); hb[5][1] = hld(5, 1);
            u32 lw1 = L_WD1 + kq * 8192 + l16, lw2 = L_WD2 + kq * 16384 + l16; PIN(lw1); PIN(lw2);
            f32x16 acc[4]; const f32x16 zero = {};
            window(0, 6, unwritten<lay>(fold_all<lay, 2>(hb[0])));
            STAMP_AT(9 + 16 * lay);
#pragma unroll
            for (int s = 0; s < 28; ++s) {
                const u32x4 a0 = s < 8 ? P[(2 * s) & 15] : s < 16 ? P2[(2 * s) & 15] : al[s & 1][0], a1 = s < 8 ? P[(2 * s + 1) & 15] : s < 16 ? P2[(2 * s + 1) & 15] : al[s & 1][1];
                const bool two = s >= 1 && s < 1 + DIST - 6;
                const int z = s < 1 ? 6 : two ? 5 + 2 * s : s + DIST;
                acc[0] = mfma(a0, hb[s][0], s == 0 ? zero : acc[0]);
                if (z < 28) hb[z][0] = hld(z, 0);
                if (two) hb[z][1] = hld(z, 1);
                SB();
                acc[2] = mfma(a1, hb[s][0], s == 0 ? zero : acc[2]);
                if (two) { hb[z + 1][0] = hld(z + 1, 0); hb[z + 1][1] = hld(z + 1, 1); } else if (z < 28) hb[z][1] = hld(z, 1);
                if (s + 1 >= 16 && s + 1 < 28) {
                    const u32 lo = s + 1 < 20 ? lw1 + 2 * (s + 1 - 16) * 1024 : lw2 + 2 * (s + 1 - 20) * 1024;
                    al[(s + 1) & 1][0] = lds_ld(lds, lo); al[(s + 1) & 1][1] = lds_ld(lds, lo + 1024);
                }
                SB();
                acc[1] = mfma(a0, hb[s][1], s == 0 ? zero : acc[1]);
                u64 bad = 0;
                if (s + 1 < 28) bad = unwritten<lay>(fold_all<lay, 2>(hb[s + 1]));
                SB();
                acc[3] = mfma(a1, hb[s][1], s == 0 ? zero : acc[3]);
                SB();
                if (s + 1 < 28) window(s + 1, two ? z + 2 : z + 1, bad);
                SB();
            }
            STAMP_AT(10 + 16 * lay);
#pragma unroll
            for (int c = 0; c < 8; ++c) lds_st(lds, L_WD1 + (kq * 8 + c) * 1024 + l16, pack8(acc[c >> 1], c & 1));
        }
        BAR();
        STAMP_AT(11 + 16 * lay);
        if (lay == 0) {
#pragma unroll
        for (int i = 0; i < 14; ++i) dma(rs, l16, OFF_WQ + (j8 * 56 + kq * 14 + i) * 1024, lds0 + L_WQ + (kq * 14 + i) * 1024);
        if (j8 < 4) {
#pragma unroll
            for (int i = 0; i < 7; ++i) dma(rs, l16, OFF_WKV + ((j8 >> 1) * 28 + kq * 7 + i) * 1024, lds0 + L_WKV + (kq * 7 + i) * 1024);
        }
        }
    }
    if (lay == 0) {
        FRESH_LANE();
        const int kq = w >> 1;
        const bool kvjob = j8 < 4 && nn == (j8 & 1), isv = j8 >> 1;
        const u32 xo = o_x2 + (ntile * 28 + 7 * kq) * 1024;
        const int sn = w < 4 ? ntile : 2 * g + (j8 & 1);
        const u32 so = o_st2 + sn * 4096, sv = (4 * h) * 512 + m * 16;
        const bool fin = w < 4 || (w == 4 && j8 < 4);
        const int kind = w < 4 ? 0 : (isv ? 2 : 1), tile = w < 4 ? 2 * j8 + mt : 16 + isv;
        u32x4 x[7], sr[4], wa[21]; f32x4 cs[4];
        asm volatile("s_barrier" ::: "memory");
#pragma unroll
        for (int i = 0; i < 7; ++i) x[i] = ldx(rs, l16, xo + i * 1024);
        asm volatile("s_waitcnt vmcnt(7)\n\ts_barrier" ::: "memory");
        if (fin) {
#pragma unroll
            for (int i = 0; i < 4; ++i) sr[i] = ldx(rs, sv, so + i * 512);
            load_cs(cs, kind, rs, tile, lane);
        }
#pragma unroll
        for (int i = 0; i < 7; ++i) { wa[i] = lds_ld(lds, L_WQ + (7 * kq + i) * 1024 + l16); wa[7 + i] = lds_ld(lds, L_WQ + (28 + 7 * kq + i) * 1024 + l16); }
        if (kvjob) {
#pragma unroll
            for (int i = 0; i < 7; ++i) wa[14 + i] = lds_ld(lds, L_WKV + (7 * kq + i) * 1024 + l16);
        }
        const Sel sel3 = make_sel(l16 >> 4);
        u32 lpe = L_PE + l16; PIN(lpe);
        SB();
        settle<2, 7>(x, unwritten<2>(fold_all<2, 7>(x)), wt, [&](int i) { return ldx(rs, l16, xo + i * 1024); });
        STAMP_AT(13 + 16 * lay);
        const f32x16 zero = {};
        f32x16 qa[2], ka;
        if (kvjob) {
            if (isv) {
#pragma unroll
                for (int i = 0; i < 7; ++i) ka = mfma(x[i], wa[14 + i], i ? ka : zero);
            } else {
#pragma unroll
                for (int i = 0; i < 7; ++i) ka = mfma(wa[14 + i], x[i], i ? ka : zero);
            }
        }
#pragma unroll
        for (int i = 0; i < 7; ++i) qa[0] = mfma(wa[i], x[i], i ? qa[0] : zero);
        if (kvjob) { lds_st(lds, lpe + (kq * 10 + 8) * 1024, pack8(ka, 0)); lds_st(lds, lpe + (kq * 10 + 9) * 1024, pack8(ka, 1)); }
#pragma unroll
        for (int i = 0; i < 7; ++i) qa[1] = mfma(wa[7 + i], x[i], i ? qa[1] : zero);
        float mean = 0.f, rstd = 0.f;
        if (fin) {
            settle<2, 4>(sr, unwritten<2>(fold_all<2, 4>(sr)), wt, [&](int i) { return ldx(rs, sv, so + i * 512); });
            stats_from(sr, mean, rstd);
        }
        PIN(mean); PIN(rstd);
#pragma unroll
        for (int c = 0; c < 4; ++c) lds_st(lds, lpe + (kq * 10 + (c >> 1) * 4 + nn * 2 + (c & 1)) * 1024, pack8(qa[c >> 1], c & 1));
        f32x16 y = {}; PIN(y);
        BAR();
        STAMP_AT(14 + 16 * lay);
        if (fin) {
        y = add_tiles<4>(sel3, lds, lpe + 2 * w * 1024, 10240, y);
        const u32 dst = w < 4 ? (u32)OFF_QF + ((g * 8 + j8) * 4 + w) * 2048 : (u32)OFF_KV + (256 + sn * 4 + (isv ? 2 : 0)) * 1024;
        qkv_finish<false>(y, kind, mean, rstd, cs, rs, ws, dst, 64 + sn, (float*)(smem + L_SC), lane);
        STAMP_AT(15 + 16 * lay);
        }
    }
    STAMP_DUMP();
}

extern "C" void kernel_launch(void* const* d_in, const int* in_sizes, int n_in, void* d_out, int out_size, void* d_ws, size_t ws_size, hipStream_t stream) {
    const uint16_t* x = (const uint16_t*)d_in[0];
    char* ws = (char*)d_ws;
    (void)hipFuncSetAttribute((const void*)k_layer<0>, hipFuncAttributeMaxDynamicSharedMemorySize, L_TOTAL);
    (void)hipFuncSetAttribute((const void*)k_layer<1>, hipFuncAttributeMaxDynamicSharedMemorySize, L_TOTAL);
    (void)hipFuncSetAttribute((const void*)k_first, hipFuncAttributeMaxDynamicSharedMemorySize, F_TOTAL);
    k_first<<<N_FIRST, 512, F_TOTAL, stream>>>(x, (const uint16_t*)d_in[1], (const uint16_t*)d_in[2], (const uint16_t*)d_in[3], (const uint16_t*)d_in[4],
                                               (const uint16_t*)d_in[6], (const uint16_t*)d_in[7], (const uint16_t*)d_in[8], ws);
    k_layer<0><<<256, 512, L_TOTAL, stream>>>(ws, x, (uint16_t*)d_out);
    k_layer<1><<<256, 512, L_TOTAL, stream>>>(ws, x, (uint16_t*)d_out);
}
```

```cpp
#include <hip/hip_runtime.h>
#include <hip/hip_bf16.h>
#include <stdint.h>
#include <type_traits>

#define FLIP 20
#define TOUCH 1
#define T45 8
#define T6 18
#define DIST 6
#define SAMEH 0
#define P3AT 4
#define PS0 19
#define PS1 2
#define RING 4
#define DSL 0
#define NOHST 0
#define CODEPF 1
#define MAP 2
#define KVSL 34
#define ARMAT 0
#define RSL 30
#define FLIP0 16
#define ARMK 0
#define TWOST 0
typedef unsigned u32;
typedef u32 u32x2 __attribute__((ext_vector_type(2)));
typedef u32 u32x4 __attribute__((ext_vector_type(4)));
typedef float f32x2 __attribute__((ext_vector_type(2)));
typedef float f32x4 __attribute__((ext_vector_type(4)));
typedef float f32x8 __attribute__((ext_vector_type(8)));
typedef float f32x16 __attribute__((ext_vector_type(16)));
typedef __bf16 bf16x2 __attribute__((ext_vector_type(2)));
typedef __bf16 bf16x8 __attribute__((ext_vector_type(8)));
typedef __bf16 bf16x16 __attribute__((ext_vector_type(16)));
#define DI __device__ __forceinline__

constexpr int S_ = 2048, D_ = 448, F_ = 1792, DH = 28;
constexpr size_t KB = 1024;
constexpr size_t OFF_WQ = 0;
constexpr size_t OFF_WKV = OFF_WQ + 448 * KB;
constexpr size_t OFF_WO = OFF_WKV + 56 * KB;
constexpr size_t OFF_WGU = OFF_WO + 512 * KB;
constexpr size_t OFF_WD = OFF_WGU + 3136 * KB;
constexpr size_t OFF_CS = OFF_WD + 1792 * KB;
constexpr size_t OFF_CG = OFF_CS + 4 * KB;
constexpr size_t OFF_QF = OFF_CG + 56 * KB;
constexpr size_t OFF_KV = OFF_QF + 2048 * KB;
constexpr size_t OFF_HO = OFF_KV + 256 * KB;
constexpr size_t OFF_X1 = OFF_HO;
constexpr size_t OFF_S1 = OFF_X1 + 1792 * KB;
constexpr size_t OFF_S2 = OFF_S1 + 256 * KB;
constexpr size_t OFF_KV2 = OFF_S2 + 256 * KB;
constexpr size_t OFF_HF = OFF_KV2 + 256 * KB;
constexpr size_t WS_END = OFF_HF + 7168 * KB;
constexpr int HO_KB = (int)((WS_END - OFF_HO) / KB);

#define STAMP_AT(i)
#define STAMP_INIT()
#define STAMP_DUMP()
DI f32x16 mfma(u32x4 a, u32x4 b, f32x16 c) {
    return __builtin_amdgcn_mfma_f32_32x32x16_bf16(__builtin_bit_cast(bf16x8, a), __builtin_bit_cast(bf16x8, b), c, 0, 0, 0);
}
DI f32x16 smfmac_hi(u32x4 a, f32x8 b, f32x16 c) {
    return __builtin_amdgcn_smfmac_f32_32x32x32_bf16(__builtin_bit_cast(bf16x8, a), __builtin_bit_cast(bf16x16, b), c, 0xDDDD, 0, 0);
}
DI u32 pk(float lo, float hi) { f32x2 v = {lo, hi}; return __builtin_bit_cast(u32, __builtin_convertvector(v, bf16x2)); }
DI float dot2(u32 a, u32 b, float c) { return __builtin_amdgcn_fdot2_f32_bf16(__builtin_bit_cast(bf16x2, a), __builtin_bit_cast(bf16x2, b), c, false); }
DI float blo(u32 x) { return __uint_as_float(x << 16); }
DI float bhi(u32 x) { return __uint_as_float(x & 0xffff0000u); }
DI float xhalf_sum(float v) { auto r = __builtin_amdgcn_permlane32_swap(__float_as_uint(v), __float_as_uint(v), false, false); return __uint_as_float(r[0]) + __uint_as_float(r[1]); }
DI float xhalf_max(float v) { auto r = __builtin_amdgcn_permlane32_swap(__float_as_uint(v), __float_as_uint(v), false, false); return fmaxf(__uint_as_float(r[0]), __uint_as_float(r[1])); }
DI u32x4 pack8(const f32x16& x, int s) { return (u32x4){pk(x[8 * s], x[8 * s + 1]), pk(x[8 * s + 2], x[8 * s + 3]), pk(x[8 * s + 4], x[8 * s + 5]), pk(x[8 * s + 6], x[8 * s + 7])}; }
DI int prow(int h, int j) { return 8 * (j >> 2) + 4 * h + (j & 3); }
DI int feat(int ks, int h, int j) {
    const int p = ks / 7, s = ks % 7;
    if (s < 6) return 112 * p + (s >= 3 ? 56 + 16 * (s - 3) : 16 * s) + prow(h, j);
    return 112 * p + 56 * (j >> 2) + 48 + 4 * h + (j & 3);
}

DI void ln_stats(float s1, float s2, float& mean, float& rstd) {
    s1 = xhalf_sum(s1); s2 = xhalf_sum(s2);
    mean = s1 * (1.f / D_);
    const float var = fmaxf(s2 * (1.f / D_) - mean * mean, 0.f);
    rstd = __builtin_amdgcn_rsqf(var + 1e-5f);
}

typedef __amdgpu_buffer_rsrc_t rsrc_t;
typedef __attribute__((address_space(3))) char* lds_t;
constexpr u32 ARMED = 0xFFFFFFFFu;
DI u32x4 ld(rsrc_t r, u32 v, u32 s) { return __builtin_amdgcn_raw_buffer_load_b128(r, v, s, 0); }
DI u32x4 ldx(rsrc_t r, u32 v, u32 s) { return __builtin_amdgcn_raw_buffer_load_b128(r, v, s, 16); }
#define HOLD(x) asm volatile("s_nop 1" ::"v"(x))
DI void st(u32x4 x, rsrc_t r, u32 v, u32 s) { __builtin_amdgcn_raw_buffer_store_b128(x, r, v, s, 0); HOLD(x); }
DI void stx(u32x4 x, rsrc_t r, u32 v, u32 s) { __builtin_amdgcn_raw_buffer_store_b128(x, r, v, s, 16); HOLD(x); }
DI void stx2(u32x2 x, rsrc_t r, u32 v, u32 s) { __builtin_amdgcn_raw_buffer_store_b64(x, r, v, s, 16); }
DI void dma(rsrc_t r, u32 v, u32 s, u32 l) { asm volatile("s_mov_b32 m0, %0\n\ts_nop 0\n\tbuffer_load_dwordx4 %1, %2, %3 offen lds" ::"s"(l), "v"(v), "s"(r), "s"(s) : "m0", "memory"); }
DI u32x4 lds_ld(lds_t p, u32 off) { return *(__attribute__((address_space(3))) u32x4*)(p + off); }
DI void lds_st(lds_t p, u32 off, u32x4 v) { *(__attribute__((address_space(3))) u32x4*)(p + off) = v; }
DI u32 umax(u32 a, u32 b) { return a > b ? a : b; }
DI u32 umin(u32 a, u32 b) { return a < b ? a : b; }
template <int MODE> DI u32 mark1(u32 a) { return MODE == 0 ? (a & ~1u) : MODE == 1 ? (a | 1u) : umin(a, ARMED - 1); }
template <int MODE> DI u32x4 mark(u32x4 a) { return (u32x4){mark1<MODE>(a[0]), mark1<MODE>(a[1]), mark1<MODE>(a[2]), mark1<MODE>(a[3])}; }
template <int MODE> DI u32 fold(u32 m, u32x4 a) {
    return MODE == 0 ? ((m | a[0] | a[1]) | a[2] | a[3]) : MODE == 1 ? ((m & a[0] & a[1]) & a[2] & a[3]) : umax(umax(umax(m, a[0]), a[1]), umax(a[2], a[3]));
}
template <int MODE, int N> DI u32 fold_all(const u32x4* x) {
    u32 m = MODE == 1 ? ~0u : 0u;
#pragma unroll
    for (int i = 0; i < N; ++i) m = fold<MODE>(m, x[i]);
    return m;
}
template <int MODE> DI bool all_written(u32 m) { return __all(MODE == 0 ? (m & 1u) == 0u : MODE == 1 ? (m & 1u) != 0u : m != ARMED); }
typedef unsigned long long u64;
template <int MODE> DI u64 unwritten(u32 m) { u64 b = __ballot(MODE == 0 ? (m & 1u) != 0u : MODE == 1 ? (m & 1u) == 0u : m == ARMED); asm volatile("" : "+s"(b)); return b; }
#define SB() __builtin_amdgcn_sched_barrier(0)
#define RAWBAR() asm volatile("s_barrier" ::: "memory")
#define PIN(x) asm volatile("" : "+v"(x))
#define BAR() asm volatile("s_waitcnt lgkmcnt(0)\n\ts_barrier" ::: "memory")
#define VM0() asm volatile("s_waitcnt vmcnt(0)" ::: "memory")
constexpr int SPIN_CAP = 1 << 15;
#define COUNT_RETRY()
struct Wait { bool dead = false; int retries = 0; };
template <int MODE, int N, class F> DI void settle(u32x4* x, u64 bad, Wait& wt, F&& reload) {
    if (__builtin_expect(bad != 0, 0)) {
        for (int sp = 0; !wt.dead; ++sp) {
            __builtin_amdgcn_s_sleep(1);
            asm volatile("" ::: "memory");
#pragma unroll
            for (int i = 0; i < N; ++i) x[i] = reload(i);
            if (all_written<MODE>(fold_all<MODE, N>(x))) break;
            if (sp > SPIN_CAP) wt.dead = true;
        }
    }
}
template <int MODE, class F> DI void gated(rsrc_t rs, u32 pv, u32 ps, Wait& wt, F&& batch) {
    for (int sp = 0;; ++sp) {
        asm volatile("" ::: "memory");
        const u32 p = __builtin_amdgcn_raw_buffer_load_b32(rs, pv, ps, 16);
        batch();
        asm volatile("" ::: "memory");
        if (__builtin_expect(all_written<MODE>(p) || wt.dead, 1)) break;
        if (sp > SPIN_CAP) wt.dead = true;
    }
}
DI u32 wave_umax(u32 v) {
    v = umax(v, __builtin_amdgcn_update_dpp(0u, v, 0x111, 0xf, 0xf, false));
    v = umax(v, __builtin_amdgcn_update_dpp(0u, v, 0x112, 0xf, 0xf, false));
    v = umax(v, __builtin_amdgcn_update_dpp(0u, v, 0x114, 0xf, 0xf, false));
    v = umax(v, __builtin_amdgcn_update_dpp(0u, v, 0x118, 0xf, 0xf, false));
    v = umax(v, __builtin_amdgcn_update_dpp(0u, v, 0x142, 0xa, 0xf, false));
    v = umax(v, __builtin_amdgcn_update_dpp(0u, v, 0x143, 0xc, 0xf, false));
    return __builtin_amdgcn_readlane(v, 63);
}

DI void load_cs(f32x4* c, int kind, rsrc_t rs, int tile, int lane) {
    if (kind != 2) {
#pragma unroll
        for (int i = 0; i < 4; ++i) c[i] = __builtin_bit_cast(f32x4, ld(rs, (lane >> 5) * 16, OFF_CS + tile * 128 + i * 32));
    } else c[0][0] = __uint_as_float(__builtin_amdgcn_raw_buffer_load_b32(rs, (lane & 31) * 4, OFF_CS + tile * 128, 0));
}
template <bool NORMED> DI void qkv_finish(f32x16 acc, int kind, float mean, float rstd, const f32x4* cs, rsrc_t rs, lds_t lds, u32 dst, float* sc, int lane) {
    const int m = lane & 31, h = lane >> 5;
    if (kind != 2) {
        const float s = kind == 0 ? rstd * (0.18898223650461362f * 1.4426950408889634f) : rstd;
#pragma unroll
        for (int i = 0; i < 4; ++i) { const f32x4 c = cs[i];
#pragma unroll
            for (int r = 0; r < 4; ++r) acc[4 * i + r] = NORMED ? ((i == 3 && h == 1) ? 0.f : acc[4 * i + r] * s) : (acc[4 * i + r] - mean * c[r]) * s; }
        u32x4 k0 = pack8(acc, 0), k1 = pack8(acc, 1);
        float nn = 0.f;
#pragma unroll
        for (int i = 0; i < 4; ++i) { nn = dot2(k0[i], k0[i], nn); nn = dot2(k1[i], k1[i], nn); }
        nn = xhalf_sum(nn);
        if (kind == 0) {
            if (h == 1) k1[2] = pk(__builtin_amdgcn_sqrtf(nn) * 1.01f, 0.f);
            if (NORMED) { st(mark<1>(k0), rs, lane * 16, dst); st(mark<1>(k1), rs, lane * 16, dst + 1024); }
            else { lds_st(lds, dst + lane * 16, k0); lds_st(lds, dst + lane * 16 + 1024, k1); }
        } else {
            const u32 mx = wave_umax(__float_as_uint(nn));
            if (h == 1) { k1[2] = 0x3f803f80u;
                          k1[3] = pk(__builtin_amdgcn_sqrtf(__uint_as_float(mx)) * 1.01f, 0.f); }
            const int slot = (m & 19) | ((m & 4) << 1) | ((m & 8) >> 1);
            if (NORMED) {
                st(k0, rs, (slot + 32 * h) * 16, dst); st(k1, rs, (slot + 32 * h) * 16, dst + 1024);
            } else {
                stx(mark<2>(k0), rs, (slot + 32 * h) * 16, dst); stx(mark<2>(k1), rs, (slot + 32 * h) * 16, dst + 1024);
            }
        }
    } else if (NORMED) {
        if (h == 0) sc[m] = rstd;
        __builtin_amdgcn_fence(__ATOMIC_RELEASE, "wavefront"); __builtin_amdgcn_wave_barrier(); __builtin_amdgcn_fence(__ATOMIC_ACQUIRE, "wavefront");
#pragma unroll
        for (int i = 0; i < 4; ++i) { const f32x4 rr = *(const f32x4*)(sc + 8 * i + 4 * h);
#pragma unroll
            for (int r = 0; r < 4; ++r) acc[4 * i + r] = m < 28 ? acc[4 * i + r] * rr[r] : (m == 28 ? 1.f : 0.f); }
        st(pack8(acc, 0), rs, lane * 16, dst); st(pack8(acc, 1), rs, lane * 16, dst + 1024);
    } else {
        if (h == 0) { sc[m] = mean; sc[32 + m] = rstd; }
        __builtin_amdgcn_fence(__ATOMIC_RELEASE, "wavefront"); __builtin_amdgcn_wave_barrier(); __builtin_amdgcn_fence(__ATOMIC_ACQUIRE, "wavefront");
        const float cv = cs[0][0];
#pragma unroll
        for (int i = 0; i < 4; ++i) { const f32x4 mu = *(const f32x4*)(sc + 8 * i + 4 * h), rr = *(const f32x4*)(sc + 32 + 8 * i + 4 * h);
#pragma unroll
            for (int r = 0; r < 4; ++r) acc[4 * i + r] = m == 28 ? 1.f : (acc[4 * i + r] - mu[r] * cv) * rr[r]; }
        stx(mark<2>(pack8(acc, 0)), rs, lane * 16, dst); stx(mark<2>(pack8(acc, 1)), rs, lane * 16, dst + 1024);
    }
}

DI void stats_from(const u32x4* s, float& mean, float& rstd) {
    float s1 = 0.f, s2 = 0.f;
#pragma unroll
    for (int i = 0; i < 4; ++i) { s1 += __uint_as_float(s[i][0]) + __uint_as_float(s[i][2]); s2 += __uint_as_float(s[i][1]) + __uint_as_float(s[i][3]); }
    ln_stats(s1, s2, mean, rstd);
}

struct Sel { u32x4 a; u32 idx; };
DI Sel make_sel(int lane) {
    asm volatile("" : "+v"(lane));
    const int m = lane & 31, grp = ((m >> 1) & 2) | ((m >> 3) & 1);
    const u32 one = (m >> 4) == (lane >> 5) ? 0x3f80u : 0u;
    Sel s; s.idx = (m & 3) * 0x5555u;
#pragma unroll
    for (int i = 0; i < 4; ++i) s.a[i] = grp == i ? one : 0u;
    PIN(s.a); PIN(s.idx);
    return s;
}
DI f32x16 add_tile(const Sel& s, u32x4 lo, u32x4 hi, f32x16 c) {
    return __builtin_amdgcn_smfmac_f32_32x32x32_bf16(__builtin_bit_cast(bf16x8, s.a), __builtin_bit_cast(bf16x16, __builtin_shufflevector(lo, hi, 0, 1, 2, 3, 4, 5, 6, 7)), c, s.idx, 0, 0);
}
template <int N> DI f32x16 add_tiles(const Sel& s, lds_t lds, u32 off, u32 stride, f32x16 acc) {
    u32x4 t[N][2];
#pragma unroll
    for (int u = 0; u < N; ++u) { t[u][0] = lds_ld(lds, off + u * stride); t[u][1] = lds_ld(lds, off + u * stride + 1024); }
    SB();
#pragma unroll
    for (int u = 0; u < N; ++u) acc = add_tile(s, t[u][0], t[u][1], acc);
    return acc;
}

typedef short s16x4 __attribute__((ext_vector_type(4)));
DI u32x4 tr8(lds_t lds, u32 pa, u32 pb) {
    const u32x2 lo = __builtin_bit_cast(u32x2, __builtin_amdgcn_ds_read_tr16_b64_v4i16((__attribute__((address_space(3))) s16x4*)(lds + pa)));
    const u32x2 hi = __builtin_bit_cast(u32x2, __builtin_amdgcn_ds_read_tr16_b64_v4i16((__attribute__((address_space(3))) s16x4*)(lds + pb)));
    return (u32x4){lo[0], lo[1], hi[0], hi[1]};
}
#define FAUX 16
#define WAUX 16
DI void stw(u32x4 x, rsrc_t r, u32 v, u32 s) { __builtin_amdgcn_raw_buffer_store_b128(x, r, v, s, WAUX); HOLD(x); }
constexpr u32 F_X = 0, F_WQ = 57344, F_WK = F_WQ + 50432, F_JOB = F_WK + 25600, F_ST = 44 * 1024, F_SC = 48 * 1024, F_TOTAL = 160 * 1024;
constexpr u32 F_UP = 14336 + 64;
constexpr u32 F_CNT = F_TOTAL - 32;
static_assert(F_JOB + F_UP + 14336 <= F_CNT, "LDS");
DI rsrc_t srd(const void* p, int bytes) { return __builtin_amdgcn_make_buffer_rsrc((void*)p, 0, bytes, 0x00020000); }
DI int fblk(int s, int half) { return s < 6 ? (s >= 3 ? 56 + 16 * (s - 3) : 16 * s) + 8 * half : 56 * half + 48; }
DI void arrive8(lds_t lds, u32 off, int lane) {
    asm volatile("s_waitcnt lgkmcnt(0)" ::: "memory");
    if (lane == 0) __hip_atomic_fetch_add((__attribute__((address_space(3))) u32*)(lds + off), 1u, __ATOMIC_RELAXED, __HIP_MEMORY_SCOPE_WORKGROUP);
    asm volatile("" ::: "memory");
}
DI void wait8(lds_t lds, u32 off, u32 n = 8u) {
    while (__builtin_amdgcn_readfirstlane(__hip_atomic_load((__attribute__((address_space(3))) u32*)(lds + off), __ATOMIC_RELAXED, __HIP_MEMORY_SCOPE_WORKGROUP)) < n) __builtin_amdgcn_s_sleep(0);
    asm volatile("" ::: "memory");
}
DI void meet8(lds_t lds, u32 off, int lane) { arrive8(lds, off, lane); wait8(lds, off); }
__global__ void __launch_bounds__(768) k_first(const uint16_t* x, const uint16_t* wq, const uint16_t* wk, const uint16_t* wv, const uint16_t* wo,
                                                const uint16_t* wg, const uint16_t* wu, const uint16_t* wd, char* ws) {
    extern __shared__ __attribute__((aligned(16))) char smem[];
    const lds_t lds = (lds_t)smem;
    const u32 lds0 = (u32)(uintptr_t)lds;
    const int b = blockIdx.x, w16 = __builtin_amdgcn_readfirstlane(threadIdx.x >> 6), w = w16 & 7, lane = threadIdx.x & 63, m = lane & 31, h = lane >> 5;
    const u32 l16 = lane * 16;
    const rsrc_t rs = srd(ws, (int)WS_END);
    const int q4 = (lane & 15) >> 2;
    const u32 c8 = ((lane >> 4) & 1) * 32 + (lane & 3) * 8;
#define FS(i)
#define FDUMP()
    FS(0);
    if (threadIdx.x < 8) *(u32*)(smem + F_CNT + threadIdx.x * 4) = 0u;
    if (__builtin_expect(w16 < 8, 1)) {
        const int g = (b & 7) * 4 + (b >> 6), j8 = (b >> 3) & 7, a2 = w >> 2, kq = w & 3;
        const bool kvwg = j8 < 4, kvjob = kvwg && w < 4, isv = j8 >> 1;
        const rsrc_t rx = srd(x, S_ * D_ * 2), rq_ = srd(wq, D_ * D_ * 2), rk_ = srd(isv ? wv : wk, D_ * DH * 2);
        const u32 xv = (lane >> 3) * 128 + (((lane & 7) ^ (lane >> 4)) * 16), wvo = (lane / 7) * 896 + (lane % 7) * 16;
        auto xpiece = [&](int i) { int p = w + 8 * i + 7 * j8; p = p >= 56 ? p - 56 : p; dma(rx, xv ^ ((p & 1) * 64), g * 57344 + p * 1024, lds0 + F_X + p * 1024); };
        auto wpiece = [&](int e) {
            int p = e + 12 * (g & 3) + ((3 * (b & 7)) >> 1); p = p >= 50 ? p - 50 : p; p = p >= 50 ? p - 50 : p;
            dma(rq_, wvo, p * 8064 + 112 * j8, lds0 + F_WQ + p * 1008); };
        auto kpiece = [&](int e) { int p = e + 3 * (2 * (g & 3) + (j8 & 1)) + ((b & 7) >= 3) + ((b & 7) >= 6); p = p >= 25 ? p - 25 : p; p = p >= 25 ? p - 25 : p;
            dma(rk_, l16, p * 1024, lds0 + F_WK + p * 1024); };
        xpiece(0);
        if (w < 2) wpiece(w);
        if (kvwg && w == 2) kpiece(0);
        BAR();
        if (TWOST) VM0();
#pragma unroll
        for (int i = 0; i < 2; ++i) wpiece(2 + w + 8 * i);
        if (kvwg) kpiece(1 + w);
#pragma unroll
        for (int i = 1; i < 7; ++i) xpiece(i);
#pragma unroll
        for (int i = 2; i < 6; ++i) wpiece(2 + w + 8 * i);
        if (kvwg) { kpiece(9 + w); kpiece(17 + w); }
        u32 xa[7], rq = F_WQ + a2 * 56 + (112 * kq + 8 * h + q4) * 112 + c8, rk = F_WK + (112 * kq + 8 * h + q4) * 56 + c8;
#pragma unroll
        for (int i = 0; i < 7; ++i) { const int ch = 14 * kq + 2 * i + h, ln = 7 * m + (ch >> 3); xa[i] = F_X + ln * 128 + 16 * ((ch ^ (ln >> 1)) & 7); PIN(xa[i]); }
        PIN(rq); PIN(rk);
        FS(1);
        VM0();
        FS(2);
        meet8(lds, F_CNT, lane);
        FS(3);
        u32x4 xf[2][7], aq[7];
#pragma unroll
        for (int i = 0; i < 7; ++i) {
            aq[i] = tr8(lds, rq + i * 1792, rq + i * 1792 + 448);
            xf[0][i] = lds_ld(lds, xa[i]); xf[1][i] = lds_ld(lds, xa[i] + 28672);
        }
        SB();
        const f32x16 zero = {};
        const u32x4 z4 = {0u, 0u, 0u, 0u};
        u32x4 pq[4], pkv[2];
        float cq = 0.f, ck = 0.f, s1 = 0.f, s2 = 0.f;
        {
            f32x16 qa[2];
#pragma unroll
            for (int i = 0; i < 7; ++i) { qa[0] = mfma(aq[i], xf[0][i], i ? qa[0] : zero); qa[1] = mfma(aq[i], xf[1][i], i ? qa[1] : zero); }
            auto sums = [&](const u32x4* xx) {
#pragma unroll
                for (int i = 0; i < 7; ++i)
#pragma unroll
                    for (int e = 0; e < 4; ++e) { cq = dot2(aq[i][e], 0x3f803f80u, cq); s1 = dot2(xx[i][e], 0x3f803f80u, s1); s2 = dot2(xx[i][e], xx[i][e], s2); }
            };
            if (a2) sums(xf[1]); else sums(xf[0]);
            if (g == 0) {
#pragma unroll
                for (int i = 0; i < 7; ++i) { const u32x4 f = tr8(lds, rq - 448 * h + fblk(i, 0) * 112, rq - 448 * h + fblk(i, 1) * 112);
                    stw(m < 28 ? f : z4, rs, l16, OFF_WQ + ((2 * j8 + a2) * 28 + 7 * kq + i) * 1024); }
            }
#pragma unroll
            for (int r = 12; r < 16; ++r) { qa[0][r] = h ? 0.f : qa[0][r]; qa[1][r] = h ? 0.f : qa[1][r]; }
#pragma unroll
            for (int c = 0; c < 4; ++c) { pq[c] = pack8(qa[c >> 1], c & 1); PIN(pq[c]); }
        }
        if (kvjob) {
            u32x4 ak[7]; f32x16 ka;
#pragma unroll
            for (int i = 0; i < 7; ++i) ak[i] = tr8(lds, rk + i * 896, rk + i * 896 + 224);
            arrive8(lds, F_CNT + 4, lane);
            if (j8 == 0) { _Pragma("unroll") for (int i = 0; i < 7; ++i) ka = mfma(ak[i], xf[0][i], i ? ka : zero); }
            else if (j8 == 1) { _Pragma("unroll") for (int i = 0; i < 7; ++i) ka = mfma(ak[i], xf[1][i], i ? ka : zero); }
            else if (j8 == 2) { _Pragma("unroll") for (int i = 0; i < 7; ++i) ka = mfma(xf[0][i], ak[i], i ? ka : zero); }
            else { _Pragma("unroll") for (int i = 0; i < 7; ++i) ka = mfma(xf[1][i], ak[i], i ? ka : zero); }
#pragma unroll
            for (int i = 0; i < 7; ++i)
#pragma unroll
                for (int e = 0; e < 4; ++e) ck = dot2(ak[i][e], 0x3f803f80u, ck);
            if (g == 1 && (j8 & 1) == 0) {
#pragma unroll
                for (int i = 0; i < 7; ++i) { const u32x4 f = tr8(lds, rk - 224 * h + fblk(i, 0) * 56, rk - 224 * h + fblk(i, 1) * 56);
                    stw(m < 28 ? f : z4, rs, l16, OFF_WKV + ((j8 >> 1) * 28 + 7 * kq + i) * 1024); }
            }
#pragma unroll
            for (int r = 12; r < 16; ++r) ka[r] = (h && !isv) ? 0.f : ka[r];
            pkv[0] = pack8(ka, 0); pkv[1] = pack8(ka, 1);
        } else arrive8(lds, F_CNT + 4, lane);
        cq = xhalf_sum(cq); ck = xhalf_sum(ck); s1 = xhalf_sum(s1); s2 = xhalf_sum(s2);
        FS(4);
        wait8(lds, F_CNT + 4);
#pragma unroll
        for (int c = 0; c < 4; ++c) lds_st(lds, (kq * 10 + a2 * 4 + c) * 1024 + l16, pq[c]);
        if (kvjob) { lds_st(lds, (kq * 10 + 8) * 1024 + l16, pkv[0]); lds_st(lds, (kq * 10 + 9) * 1024 + l16, pkv[1]); }
        *(f32x2*)(smem + F_ST + w * 256 + m * 8) = (f32x2){s1, s2};
        *(float*)(smem + F_SC + w * 128 + m * 4) = m < 28 ? cq : 0.f;
        if (w < 4) *(float*)(smem + F_SC + (8 + w) * 128 + m * 4) = m < 28 ? ck : 0.f;
        const Sel sel = make_sel(lane);
        meet8(lds, F_CNT + 8, lane);
        FS(5);
        if (w < 4 || (w == 4 && kvwg)) {
            const int n = w < 4 ? (w & 1) : (j8 & 1), ct = w < 4 ? (w >> 1) : 2, kind = w < 4 ? 0 : (isv ? 2 : 1);
            s1 = 0.f; s2 = 0.f;
#pragma unroll
            for (int u = 0; u < 4; ++u) { const f32x2 t = *(const f32x2*)(smem + F_ST + (n * 4 + u) * 256 + m * 8); s1 += t[0]; s2 += t[1]; }
            const float c0 = *(const float*)(smem + F_SC + (ct * 4 + 2 * h) * 128 + m * 4), c1 = *(const float*)(smem + F_SC + (ct * 4 + 2 * h + 1) * 128 + m * 4);
            f32x16 y = add_tiles<4>(sel, lds, 2 * w * 1024 + l16, 10240, zero);
            const float nm = s1 * (-1.f / D_), rstd = __builtin_amdgcn_rsqf(fmaxf(s2 * (1.f / D_) - nm * nm, 0.f) + 1e-5f);
            const u32 mh = pk(nm, 0.f), mm = mh | (pk(nm - blo(mh), 0.f) << 16), h0 = pk(c0, c0), h1 = pk(c1, c1);
            const u32x4 fm = {mm, mm, mm, mm}, fc = {h0, pk(c0 - blo(h0), c0 - blo(h0)), h1, pk(c1 - blo(h1), c1 - blo(h1))};
            y = kind == 2 ? mfma(fm, fc, y) : mfma(fc, fm, y);
            const u32 dst = w < 4 ? (u32)OFF_QF + ((g * 8 + j8) * 4 + w) * 2048 : (u32)OFF_KV + ((2 * g + n) * 4 + (isv ? 2 : 0)) * 1024;
            qkv_finish<true>(y, kind, 0.f, rstd, nullptr, rs, lds, dst, (float*)(smem + F_SC + 1536), lane);
        }
        FS(6);
        if (w == 7 && (g == 0 || (g == 1 && (j8 & 5) == 0 && h == 0))) {
            const float* p = (const float*)(smem + F_SC) + (g == 0 ? h : 2) * 128 + m;
            ((float*)(ws + OFF_CS))[(g == 0 ? 2 * j8 + h : 16 + (j8 >> 1)) * 32 + m] = (p[0] + p[32]) + (p[64] + p[96]);
        }
    } else {
        const int job = b;
        const bool isg = job < 112, isd = !isg && job < 168;
        const int jb = isg ? job : isd ? job - 112 : job - 168, tq = jb >> 2, p4 = jb & 3;
        BAR();
        if (w >= 2) {
            u32x4 a4 = {ARMED, ARMED, ARMED, ARMED}; PIN(a4);
            if (CODEPF) {
                u64 pc;
                asm volatile("s_getpc_b64 s[20:21]\n\ts_add_u32 s20, s20, _Z6k_mainPcPKtPt@rel32@lo+4\n\ts_addc_u32 s21, s21, _Z6k_mainPcPKtPt@rel32@hi+12\n\ts_mov_b64 %0, s[20:21]" : "=s"(pc) :: "s20", "s21", "scc");
                const int i = (b >> 3) * 2 + (w - 2);
                u32x4 c = *(const __attribute__((address_space(1))) u32x4*)(pc + i * 1024 + l16), d = *(const __attribute__((address_space(1))) u32x4*)(pc + (64 + (i & 15)) * 1024 + l16);
                PIN(c); PIN(d);
            }
#pragma unroll 1
            for (int e = b * 2 + (w - 2); e < HO_KB; e += 512) stx(a4, rs, l16, OFF_HO + e * 1024);
        } else {
        __builtin_amdgcn_s_sleep(RSL);
        if (isg) {
            const rsrc_t rg = srd(w ? wu : wg, D_ * F_ * 2);
            const u32 gv = (lane >> 3) * 3584 + (lane & 7) * 16;
#pragma unroll
            for (int p = 0; p < 14; ++p) dma(rg, gv, (112 * p4 + 8 * p) * 3584 + 128 * tq, lds0 + F_JOB + w * F_UP + p * 1024);
        } else if (job < 184) {
            const rsrc_t rd = srd(isd ? wd : wo, isd ? F_ * D_ * 2 : D_ * D_ * 2);
#pragma unroll
            for (int i = 0; i < 14; ++i) { const int p = w + 2 * i; if (p < (isd ? 28 : 25)) dma(rd, l16, jb * (isd ? 28672 : 25088) + p * 1024, lds0 + F_JOB + p * 1024); }
        }
        FS(1);
        VM0();
        FS(2);
        arrive8(lds, F_CNT + 16, lane); wait8(lds, F_CNT + 16, 2u);
        FS(3);
#pragma unroll
        for (int r = 0; r < 2; ++r) {
            const int v = 2 * w + r;
            if (isg) {
                const u32 base = F_JOB + ((lane >> 4) & 1) * F_UP + v * 32 + (lane & 3) * 8 + (4 * h + q4) * 128;
                u32x4 f[7]; float cs = 0.f;
#pragma unroll
                for (int i = 0; i < 7; ++i) f[i] = tr8(lds, base + fblk(i, 0) * 128, base + fblk(i, 1) * 128);
#pragma unroll
                for (int i = 0; i < 7; ++i) { stw(f[i], rs, l16, OFF_WGU + ((4 * tq + v) * 28 + 7 * p4 + i) * 1024);
#pragma unroll
                    for (int e = 0; e < 4; ++e) cs = dot2(f[i][e], 0x3f803f80u, cs); }
                cs = xhalf_sum(cs);
                if (h == 0) ((float*)(ws + OFF_CG))[(p4 * 112 + 4 * tq + v) * 32 + m] = cs;
            } else if (job < 184) {
                u32x4 f[8];
#pragma unroll
                for (int i = 0; i < 8; ++i) {
                    const u32 a0 = F_JOB + (16 * (i & 1) + 4 * h + q4) * 896 + 112 * (v + 4 * (i >> 2)) + 64 * ((i >> 1) & 1) + c8;
                    f[i] = tr8(lds, a0, a0 + 8 * 896);
                }
#pragma unroll
                for (int i = 0; i < 8; ++i) {
                    const int jm = (v + 4 * (i >> 2)) * 2 + ((i >> 1) & 1), sk = i & 1;
                    if ((i & 2) && m >= 24) f[i] = (u32x4){0u, 0u, 0u, 0u};
                    if (!isd && sk == 1 && h == 1) { f[i][2] = 0u; f[i][3] = 0u; }
                    stw(f[i], rs, l16, isd ? OFF_WD + (jm * 112 + 2 * jb + sk) * 1024 : OFF_WO + (jm * 32 + 2 * jb + sk) * 1024);
                }
            }
        }
        }
        FS(4);
    }
    FDUMP();
}

constexpr u32 L_RED = 0, L_GU0 = 64 * 1024, L_GU3 = 32 * 1024, L_GU4 = 0, L_GU5 = 64 * 1024, L_GU6 = 92 * 1024, L_WD1 = 120 * 1024, L_WD2 = 0,
              L_CS = 152 * 1024, L_SC = 154 * 1024, L_KN = 155 * 1024, L_WQ = 0, L_WKV = 56 * 1024, L_PE = 84 * 1024, L_TOTAL = 160 * 1024;

template <int lay> DI void layer(char* ws, const uint16_t* xin, uint16_t* out, char* smem, Wait& wt) {
    const lds_t lds = (lds_t)smem;
    const u32 lds0 = (u32)(uintptr_t)lds;
    int b = blockIdx.x, w = __builtin_amdgcn_readfirstlane(threadIdx.x >> 6);
    asm volatile("" : "+s"(b), "+s"(w));
    const int g = (b & 7) * 4 + (b >> 6), j8 = (b >> 3) & 7;
    const int blk1 = MAP == 2 ? (b & 1) * 8 + ((b >> 3) & 7) : MAP == 1 ? ((b >> 1) & 3) * 4 + ((b >> 3) & 3) : g >> 1;
    const int i16 = MAP == 2 ? ((b >> 1) & 3) * 4 + (b >> 6) : MAP == 1 ? (b & 1) * 8 + (b >> 5) : (b >> 3) & 15;
    u32 l16 = __builtin_amdgcn_mbcnt_hi(~0u, __builtin_amdgcn_mbcnt_lo(~0u, 0u)) * 16; PIN(l16);
#define FRESH_LANE() u32 l16_ = l16; PIN(l16_); const int lane = l16_ >> 4, m = lane & 31, h = lane >> 5; (void)m; (void)h
    const rsrc_t rs = __builtin_amdgcn_make_buffer_rsrc(ws, 0, (int)WS_END, 0x00020000);
    constexpr u32 o_x1 = OFF_X1, o_x2 = OFF_X1, o_st1 = OFF_S1, o_st2 = OFF_S2, o_hf = OFF_HF;
    const int mt = (w >> 1) & 1, nn = w & 1, ntile = 2 * g + nn;
    const int ks0 = 7 * (j8 >> 1) + 3 * (j8 & 1), ksh = 7 * (j8 >> 1) + 6;
    const u32 xo0 = (ntile * 28 + ks0 + 2 * mt) * 1024, xo1 = mt ? (ntile * 28 + ksh) * 1024 + 8 * (j8 & 1) : xo0 + 1024;
    const u32 sto = ntile * 4096 + j8 * 512 + mt * 8;
    STAMP_INIT();
    STAMP_AT(0 + 16 * lay);

    const u32 po = w < 4 ? OFF_WO + (j8 * 64 + 8 * w) * 1024 : OFF_WD + (j8 * 224 + 28 * (w - 4)) * 1024;
    const u32 l16o = l16;
    auto attend = [&](auto role, u32x4* P) {
        constexpr bool A = decltype(role)::value;
        u32 l16 = l16o;
        if (A) asm volatile("; waves 0-3" : "+v"(l16)); else asm volatile("; waves 4-7" : "+v"(l16));
    u32 kvo = (lay ? OFF_KV2 : OFF_KV) + w * 32 * 1024, pb = po, gb = OFF_WGU + (i16 * 196 + w * 21) * 1024, gl = lds0 + L_GU0 + w * 21 * 1024;
    auto kvld = [&](u32 off) { return lay ? ldx(rs, l16, kvo + off) : ld(rs, l16, kvo + off); };
    auto first_keys = [&](u32x4 (*kf)[4]) {
#pragma unroll
        for (int i = 0; i < 4; ++i) kf[0][i] = kvld(i * 1024);
        kf[1][0] = kvld(4096);
    };
    auto keys_complete = [&]() {
        for (int sp = 0; !wt.dead; ++sp) {
            u32 mx = 0;
            asm volatile("" ::: "memory");
#pragma unroll 1
            for (int i = 0; i < 32; i += 4) { u32x4 t[4];
#pragma unroll
                for (int e = 0; e < 4; ++e) t[e] = kvld((i + e) * 1024);
                mx = umax(mx, fold_all<2, 4>(t)); }
            if (all_written<2>(mx)) break;
            if (sp > SPIN_CAP) wt.dead = true;
        }
    };
    if (!A) __builtin_amdgcn_s_setprio(1);
    u32x4 q[4][2], kf[2][4];
    if (lay == 0) dma(rs, l16, OFF_QF + ((g * 8 + j8) * 8 + w) * 1024, lds0 + w * 1024);
    const u32 pv = ((l16 >> 4) & 7) * 4096 + ((l16 & 128) ? 3072 : 1024 + 32 * 16 + 12);
    u32 knv;
    for (int sp = 0;; ++sp) {
        asm volatile("" ::: "memory");
        knv = __builtin_amdgcn_raw_buffer_load_b32(rs, pv, kvo, lay ? 16 : 0);
        SB();
        first_keys(kf);
        asm volatile("" ::: "memory");
        if (lay == 0 || __builtin_expect(all_written<2>(fold<2>(umax(knv, fold_all<2, 4>(kf[0])), kf[1][0])) || wt.dead, 1)) break;
        if (sp > SPIN_CAP) wt.dead = true;
    }
    if (l16 < 128) *(u32*)(smem + L_KN + w * 32 + (l16 >> 2)) = knv << 16;
    if (lay == 0) asm volatile("s_waitcnt vmcnt(6)" ::: "memory");
    BAR();
#pragma unroll
    for (int i = 0; i < 8; ++i) q[i >> 1][i & 1] = lds_ld(lds, i * 1024 + l16);
    knv = *(const u32*)(smem + L_KN + (l16 >> 2));
    bool checked = lay == 0;
    {
        FRESH_LANE();
        const float kn = __uint_as_float(wave_umax(knv));
        float sh[4]; bool safe = true;
#pragma unroll
        for (int t = 0; t < 4; ++t) { sh[t] = blo(q[t][1][2]) * kn; safe = safe && (h == 0 || sh[t] <= 48.f); }
        if (__builtin_expect(!__all(safe), 0)) {
            if (lay == 1) { keys_complete(); first_keys(kf); checked = true; }
            float* mx = (float*)(smem + 8192);
            float rm[4] = {-INFINITY, -INFINITY, -INFINITY, -INFINITY};
#pragma unroll
            for (int t = 0; t < 4; ++t) if (h == 1) q[t][1][2] = 0u;
#pragma unroll 1
            for (int kt = 0; kt < 8; ++kt) {
                const u32x4 k0 = kvld(kt * 4096), k1 = kvld(kt * 4096 + 1024);
#pragma unroll
                for (int t = 0; t < 4; ++t) {
                    f32x16 s = {}; s = mfma(k0, q[t][0], s); s = mfma(k1, q[t][1], s);
#pragma unroll
                    for (int r = 0; r < 16; ++r) rm[t] = fmaxf(rm[t], s[r]);
                }
            }
#pragma unroll
            for (int t = 0; t < 4; ++t) { rm[t] = xhalf_max(rm[t]); if (h == 0) mx[(w * 4 + t) * 32 + m] = rm[t]; }
            __syncthreads();
#pragma unroll
            for (int t = 0; t < 4; ++t) { float v = mx[t * 32 + m];
#pragma unroll
                for (int u = 1; u < 8; ++u) v = fmaxf(v, mx[(u * 4 + t) * 32 + m]);
                sh[t] = v; }
            __syncthreads();
        }
#pragma unroll
        for (int t = 0; t < 4; ++t) {
            const u32 hi = pk(-sh[t], 0.f) & 0xffffu; const u32 lo = pk(-sh[t] - blo(hi), 0.f);
            if (h == 1) q[t][1][2] = hi | (lo << 16);
        }
    }
    STAMP_AT(1 + 16 * lay);
        f32x16 o[4];
        for (;;) {
        asm volatile("" : "+s"(kvo), "+s"(pb), "+s"(gb), "+s"(gl));
#pragma unroll
        for (int t = 0; t < 4; ++t) o[t] = f32x16{};
        f32x16 sb[3];
        const f32x16 zero = {};
        sb[0] = mfma(kf[0][0], q[0][0], zero); sb[0] = mfma(kf[0][1], q[0][1], sb[0]);
#define EX1(x) do { float t_ = __builtin_amdgcn_exp2f(x); PIN(t_); x = t_; } while (0)
#define EX4(S, i) do { EX1(S[i]); EX1(S[i + 1]); EX1(S[i + 2]); EX1(S[i + 3]); SB(); } while (0)
        SB();
#pragma unroll
        for (int u = 0; u < 32; ++u) {
            const int kt = u >> 2, t = u & 3, un = u + 1, ktn = un >> 2, tn = un & 3, up = u - 1;
            f32x16& cur = sb[u % 3]; f32x16& nxt = sb[un % 3]; f32x16& prv = sb[(u + 2) % 3];
            if (un < 32) nxt = mfma(kf[ktn & 1][0], q[tn][0], zero);
            SB();
            if (u < 6 && A) {
#pragma unroll
                for (int i = 3 * u; i < 3 * u + 3 && i < 14; ++i) P[i] = ld(rs, l16, pb + ((i >> 3) * 32 + (i & 7)) * 1024);
            }
            SB(); EX4(cur, 0);
            if (un < 32) nxt = mfma(kf[ktn & 1][1], q[tn][1], nxt);
            SB(); EX4(cur, 4);
            if (up >= 0) o[up & 3] = smfmac_hi(kf[(up >> 2) & 1][3], __builtin_shufflevector(prv, prv, 8, 9, 10, 11, 12, 13, 14, 15), o[up & 3]);
            SB();
            if (t == 3) { if (kt + 2 < 8) kf[kt & 1][0] = kvld((kt + 2) * 4096); }
            else if (kt + 1 < 8) kf[(kt + 1) & 1][t + 1] = kvld(((kt + 1) * 4 + t + 1) * 1024);
            SB(); EX4(cur, 8);
            o[t] = smfmac_hi(kf[kt & 1][2], __builtin_shufflevector(cur, cur, 0, 1, 2, 3, 4, 5, 6, 7), o[t]);
            SB();
            if (u < 7 && A) {
#pragma unroll
                for (int p = 3 * u; p < 3 * u + 3; ++p) dma(rs, l16, gb + p * 1024, gl + p * 1024);
            }
            if (u >= 26 && !A) {
#pragma unroll
                for (int i = 3 * (u - 26); i < 3 * (u - 26) + 3 && i < 16; ++i) P[i] = ld(rs, l16, pb + ((i & 1) * 112 + 7 * (((i >> 1) + j8) & 3) + (i >> 3)) * 1024);
            }
            if (u == FLIP && !A) __builtin_amdgcn_s_setprio(0);
            SB(); EX4(cur, 12);
        }
        o[3] = smfmac_hi(kf[1][3], __builtin_shufflevector(sb[31 % 3], sb[31 % 3], 8, 9, 10, 11, 12, 13, 14, 15), o[3]);
        if (lay == 0) break;
        float pz = o[0][0];
#pragma unroll
        for (int r = 1; r < 16; ++r) pz += o[0][r];
        if (__builtin_expect(checked || !__any(pz != pz), 1)) break;
        keys_complete(); first_keys(kf); checked = true;
        }
        STAMP_AT(2 + 16 * lay);
#pragma unroll
        for (int c = 0; c < 8; ++c) lds_st(lds, L_RED + (w * 8 + c) * 1024 + l16, pack8(o[c >> 1], c & 1));
    };
    if (w < 4) {
        u32x4 P[16], res[2];
        attend(std::true_type{}, P);
        SB();
        P[14] = ld(rs, l16, po + 38 * 1024); P[15] = ld(rs, l16, po + 39 * 1024);
        if (lay) { res[0] = ldx(rs, l16, o_x2 + xo0); res[1] = ldx(rs, l16, o_x2 + (xo1 & ~15u)); }
        else {
            FRESH_LANE();
            const uint16_t* r = xin + (size_t)(32 * ntile + m) * D_ + 56 * j8 + 32 * mt + 4 * h;
            const u32x2 r0 = *(const u32x2*)r, r1 = *(const u32x2*)(r + 8), r2 = *(const u32x2*)(r + 16), r3 = *(const u32x2*)(r + (mt ? 16 : 24));
            res[0] = (u32x4){r0[0], r0[1], r1[0], r1[1]}; res[1] = (u32x4){r2[0], r2[1], r3[0], r3[1]};
        }
        SB();
        const Sel sel = make_sel(l16 >> 4);
        f32x16 acc0 = {}; PIN(acc0);
        BAR();
        STAMP_AT(3 + 16 * lay);
        {
            f32x16 acc = add_tiles<8>(sel, lds, L_RED + 2 * w * 1024 + l16, 8192, acc0);
            BAR();
            const auto sw = __builtin_amdgcn_permlane32_swap(__float_as_uint(acc[12]), __float_as_uint(acc[12]), false, false);
            const float il = __builtin_amdgcn_rcpf(__uint_as_float(sw[1]));
#pragma unroll
            for (int r = 0; r < 16; ++r) acc[r] *= il;
            const u32 oo = OFF_QF + ((g * 8 + j8) * 8 + 2 * w) * 1024;
            stx(mark<lay>(pack8(acc, 0)), rs, l16, oo); stx(mark<lay>(pack8(acc, 1)), rs, l16, oo + 1024);
        }
        STAMP_AT(4 + 16 * lay);
        VM0();
        STAMP_AT(5 + 16 * lay);
        u32x4 xr[2];
        {
            u32x4 ob[16];
            const u32 oo = OFF_QF + (g * 8 + 2 * w) * 8192;
            auto old = [&](int i) { const int e = (i >> 2) * 4 + (i & 1) * 2 + ((i >> 1) & 1); return ldx(rs, l16 + (e & 3) * 1024, oo + (e >> 2) * 4096); };
            gated<lay>(rs, ((l16 >> 4) & 7) * 2048 + 1024, oo, wt, [&]() {
#pragma unroll
                for (int i = 0; i < 10; ++i) ob[i] = old(i); });
#pragma unroll
            for (int i = 10; i < 16; ++i) ob[i] = old(i);
            if (mt) res[1] = (lay && (j8 & 1)) ? (u32x4){res[1][2], res[1][3], 0u, 0u} : (u32x4){res[1][0], res[1][1], 0u, 0u};
            f32x16 y0 = add_tile(sel, res[0], res[1], f32x16{}); PIN(y0);
            f32x16 acc[4] = {};
#pragma unroll
            for (int c = 0; c < 4; ++c) {
                settle<lay, 4>(ob + 4 * c, unwritten<lay>(fold_all<lay, 4>(ob + 4 * c)), wt, [&](int i) { return old(4 * c + i); });
#pragma unroll
                for (int i = 0; i < 4; ++i) { const int ks = 2 * c + (i >> 1), n = i & 1;
                    acc[n] = mfma(P[ks], ob[4 * c + i], acc[n]); acc[2 + n] = mfma(P[8 + ks], ob[4 * c + i], acc[2 + n]); }
            }
            STAMP_AT(6 + 16 * lay);
#pragma unroll
            for (int c = 0; c < 8; ++c) lds_st(lds, (w * 8 + c) * 1024 + l16, pack8(acc[c >> 1], c & 1));
            BAR();
            f32x16 y = add_tiles<4>(sel, lds, 2 * w * 1024 + l16, 8192, y0);
            BAR();
            xr[0] = pack8(y, 0); xr[1] = pack8(y, 1);
            stx(mark<0>(xr[0]), rs, l16, o_x1 + xo0);
            if (mt) stx2((u32x2){mark1<0>(xr[1][0]), mark1<0>(xr[1][1])}, rs, l16, o_x1 + xo1); else stx(mark<0>(xr[1]), rs, l16, o_x1 + xo1);
            float s1 = 0.f, s2 = 0.f;
#pragma unroll
            for (int r = 0; r < 16; ++r) { s1 += y[r]; s2 = fmaf(y[r], y[r], s2); }
            s1 = xhalf_sum(s1); s2 = xhalf_sum(s2);
            if (l16 < 512) stx2((u32x2){mark1<lay>(__float_as_uint(s1)), mark1<lay>(__float_as_uint(s2))}, rs, l16, o_st1 + sto);
        }
        STAMP_AT(7 + 16 * lay);
        {
            __builtin_amdgcn_s_setprio(2);
            FRESH_LANE();
            const int nt = 4 * blk1 + w;
            const u32 xo = o_x1 + nt * 28 * 1024, so = o_st1 + nt * 4096 + 0u, sv = (4 * h) * 512 + m * 16;
            u32x4 x[28], sr[4];
            f32x16 a[4]; const f32x16 zero = {};
            u32x4 ar[6], br[RING];
            u32 lb = l16 + L_GU0, lc = l16 + L_GU0 + 56 * 1024, lcs = L_CS + h * 16; PIN(lb); PIN(lc); PIN(lcs);
            auto gu012 = [&](int tt, int ks) { return tt < 2 ? lds_ld(lds, lb + (tt * 28 + ks) * 1024) : lds_ld(lds, lc + ks * 1024); };
            auto gu36 = [&](int e) { return e < 28 ? lds_ld(lds, l16 + L_GU3 + e * 1024) : e < 56 ? lds_ld(lds, l16 + L_GU4 + (e - 28) * 1024) : lds_ld(lds, lb + (e - 56) * 1024); };
#pragma unroll
            for (int i = 0; i < 6; ++i) ar[i] = gu012(i % 3, i / 3);
            const int pj = lane & 7;
            u32 pv = (lane & 8) ? (7 * (pj >> 1) + 6) * 1024 + 8 * (pj & 1) : (7 * (pj >> 1) + 3 * (pj & 1) + 1) * 1024; PIN(pv);
            SB();
            asm volatile("s_waitcnt vmcnt(1)" ::: "memory");
            STAMP_AT(8 + 16 * lay);
            gated<0>(rs, pv, xo, wt, [&]() {
#pragma unroll
                for (int ks = 0; ks < 10; ++ks) x[ks] = ldx(rs, l16 + (ks & 3) * 1024, xo + (ks >> 2) * 4096); });
            settle<0, 2>(x, unwritten<0>(fold_all<0, 2>(x)), wt, [&](int i) { return ldx(rs, l16, xo + i * 1024); });
            x[10] = ldx(rs, l16 + 2048, xo + 8192); x[11] = ldx(rs, l16 + 3072, xo + 8192);
            float nmean = 0.f;
            SB();
#pragma unroll
            for (int c = 0; c < 14; ++c) {
                u32 mx = 0; u64 bad = 0, bads = 0;
#pragma unroll
                for (int i = 0; i < 6; ++i) {
                    const int ks = 2 * c + i / 3, tt = i % 3, e = ks * 3 + tt;
                    a[tt] = mfma(ar[e % 6], x[ks], ks == 0 ? zero : a[tt]);
                    if (ks + 2 < 28) ar[e % 6] = gu012(tt, ks + 2); else if (i < RING) br[i] = gu36(i);
                    if (tt == 0) { if (ks + 12 < 28) x[ks + 12] = ldx(rs, l16 + (ks & 3) * 1024, xo + ((ks + 12) >> 2) * 4096); else if (ks < 20) sr[ks - 16] = ldx(rs, sv, so + (ks - 16) * 512); }
                    if (c < 13) {
                        if (i == 3) { mx = fold_all<0, 2>(x + 2 * c + 2); PIN(mx); }
                        if (i == 4) bad = unwritten<0>(mx);
                        if (c == 12 && i == 1) { mx = fold_all<lay, 4>(sr); PIN(mx); }
                        if (c == 12 && i == 2) bads = unwritten<lay>(mx);
                    } else {
                        if (i == 0) { nmean = (__uint_as_float(sr[0][0]) + __uint_as_float(sr[0][2])) + (__uint_as_float(sr[1][0]) + __uint_as_float(sr[1][2])); PIN(nmean); }
                        if (i == 1) { nmean += (__uint_as_float(sr[2][0]) + __uint_as_float(sr[2][2])) + (__uint_as_float(sr[3][0]) + __uint_as_float(sr[3][2])); PIN(nmean); }
                        if (i == 2) { nmean = xhalf_sum(nmean) * (-1.f / D_); PIN(nmean); }
                    }
                    SB();
                }
                if (c < 13) settle<0, 2>(x + 2 * c + 2, bad, wt, [&](int i) { return ldx(rs, l16, xo + (2 * c + 2 + i) * 1024); });
                if (c == 12) settle<lay, 4>(sr, bads, wt, [&](int i) { return ldx(rs, sv, so + i * 512); });
                SB();
            }
            STAMP_AT(9 + 16 * lay);
            f32x4 cs[2][4]; float hv[2][8]; u32x4 hp[2];
            auto epi = [&](int z, int e, int k, f32x16& v) {
                if (k < 2) { cs[z][2 * k] = __builtin_bit_cast(f32x4, lds_ld(lds, lcs + e * 128 + (2 * k) * 32)); cs[z][2 * k + 1] = __builtin_bit_cast(f32x4, lds_ld(lds, lcs + e * 128 + (2 * k + 1) * 32)); }
                else if (k >= 3 && k < 11) { const int r = k - 3;
                    float gg = fmaf(nmean, cs[z][r >> 2][r & 3], v[r]), uu = v[r + 8];
                    asm volatile("" : "+v"(gg), "+v"(uu));
                    hv[z][r] = gg * fmaf(nmean, cs[z][2 + (r >> 2)][r & 3], uu); PIN(hv[z][r]); }
                else if (k == 11) { hp[z][0] = mark1<lay>(pk(hv[z][0], hv[z][1])); hp[z][1] = mark1<lay>(pk(hv[z][2], hv[z][3])); PIN(hp[z]); }
                else if (k == 12) { hp[z][2] = mark1<lay>(pk(hv[z][4], hv[z][5])); hp[z][3] = mark1<lay>(pk(hv[z][6], hv[z][7])); PIN(hp[z]); }
                else if (k == 13) stx(hp[z], rs, l16, o_hf + (nt * 112 + 7 * i16 + e) * 1024);
            };
#pragma unroll
            for (int tt = 3; tt < 7; ++tt) {
#pragma unroll
                for (int ks = 0; ks < 28; ++ks) {
                    const int e = (tt - 3) * 28 + ks;
                    a[tt & 3] = mfma(br[e % RING], x[ks], ks == 0 ? zero : a[tt & 3]);
                    if (e == 2) RAWBAR();
                    if (e == 56 - RING) RAWBAR();
                    if (e == 68) RAWBAR();
                    if (e + RING < 112) br[e % RING] = gu36(e + RING);
                    if (tt == 3) epi(ks / 14, ks / 14, ks % 14, a[ks / 14]);
                    else if (tt == 4) epi(ks / 14, 2 + ks / 14, ks % 14, a[2 + ks / 14]);
                    else if (ks >= 1 && ks < 15) epi(0, tt - 1, ks - 1, a[(tt - 1) & 3]);
                    else if (tt == 6 && ks >= 26) epi(0, 6, ks - 26, a[2]);
                    SB();
                }
            }
#pragma unroll
            for (int k = 2; k < 14; ++k) epi(0, 6, k, a[2]);
            __builtin_amdgcn_s_setprio(0);
        }
        STAMP_AT(10 + 16 * lay);
        const Sel sel2 = make_sel(l16 >> 4);
        float r2; f32x16 y0 = {}, xres;
        { FRESH_LANE();
          const u32 so = o_st1 + ntile * 4096, sv = (4 * h) * 512 + m * 16;
          u32x4 sr[4];
#pragma unroll
          for (int i = 0; i < 4; ++i) sr[i] = ldx(rs, sv, so + i * 512);
          settle<lay, 4>(sr, unwritten<lay>(fold_all<lay, 4>(sr)), wt, [&](int i) { return ldx(rs, sv, so + i * 512); });
          float mean, rstd; stats_from(sr, mean, rstd); r2 = rstd * rstd;
#pragma unroll
          for (int i = 0; i < 8; ++i) { xres[2 * i] = blo(xr[i >> 2][i & 3]); xres[2 * i + 1] = bhi(xr[i >> 2][i & 3]); } }
        u32 lpd = L_WD1 + 2 * w * 1024 + l16; PIN(lpd); PIN(r2); PIN(y0); PIN(xres);
        BAR();
        STAMP_AT(11 + 16 * lay);
        {
            f32x16 y = add_tiles<4>(sel2, lds, lpd, 8192, y0);
#pragma unroll
            for (int r = 0; r < 16; ++r) y[r] = fmaf(y[r], r2, xres[r]);
            if (lay == 1) {
                FRESH_LANE();
                const u32x4 p0 = pack8(y, 0), p1 = pack8(y, 1);
                uint16_t* r = out + (size_t)(32 * ntile + m) * D_ + 56 * j8 + 32 * mt + 4 * h;
                *(u32x2*)r = (u32x2){p0[0], p0[1]}; *(u32x2*)(r + 8) = (u32x2){p0[2], p0[3]}; *(u32x2*)(r + 16) = (u32x2){p1[0], p1[1]};
                if (mt == 0) *(u32x2*)(r + 24) = (u32x2){p1[2], p1[3]};
            } else {
            const u32x4 p0 = mark<1>(pack8(y, 0)), p1 = mark<1>(pack8(y, 1));
            stx(p0, rs, l16, o_x2 + xo0);
            if (mt) stx2((u32x2){p1[0], p1[1]}, rs, l16, o_x2 + xo1); else stx(p1, rs, l16, o_x2 + xo1);
            float s1 = 0.f, s2 = 0.f;
#pragma unroll
            for (int r = 0; r < 16; ++r) { s1 += y[r]; s2 = fmaf(y[r], y[r], s2); }
            s1 = xhalf_sum(s1); s2 = xhalf_sum(s2);
            if (l16 < 512) stx2((u32x2){mark1<2>(__float_as_uint(s1)), mark1<2>(__float_as_uint(s2))}, rs, l16, o_st2 + sto);
            }
        }
        STAMP_AT(12 + 16 * lay);
        if (lay == 0) asm volatile("s_waitcnt vmcnt(1)" ::: "memory");
    } else {
        u32x4 P[16], P2[16];
        attend(std::false_type{}, P);
        const int kq = w - 4;
        const u32 gu = OFF_WGU + (i16 * 196 + kq * 7) * 1024, wdo = OFF_WD + (j8 * 224 + 28 * kq) * 1024;
        auto wdf = [&](int i, int t0) { return ld(rs, l16, wdo + ((i & 1) * 112 + 7 * (((i >> 1) + j8) & 3) + t0 + (i >> 3)) * 1024); };
        BAR();
        STAMP_AT(3 + 16 * lay);
#pragma unroll
        for (int i = 0; i < 8; ++i) P2[i] = wdf(i, 2);
        BAR();
#pragma unroll
        for (int i = 0; i < 7; ++i) dma(rs, l16, gu + (84 + i) * 1024, lds0 + L_GU3 + (kq * 7 + i) * 1024);
        if (lay == 0) { float cs = 0.f;
#pragma unroll
          for (int p = 0; p < 4; ++p) cs += __uint_as_float(__builtin_amdgcn_raw_buffer_load_b32(rs, l16 >> 2, OFF_CG + (p * 112 + 7 * i16) * 128 + kq * 224, 0));
          if (l16 < 56 * 16) *(float*)(smem + L_CS + kq * 224 + (l16 >> 2)) = cs; }
        VM0();
        BAR();
        u32x4 P3[16];
#pragma unroll
        for (int i = 8; i < 16; ++i) P2[i] = wdf(i, 2);
        BAR();
#pragma unroll
        for (int i = 0; i < 7; ++i) dma(rs, l16, gu + (112 + i) * 1024, lds0 + L_GU4 + (kq * 7 + i) * 1024);
#pragma unroll
        for (int i = 0; i < 4; ++i) P3[i] = wdf(i, 5);
        __builtin_amdgcn_s_sleep(PS0);
#pragma unroll
        for (int i = 4; i < 16; ++i) { P3[i] = wdf(i, 5); SB(); __builtin_amdgcn_s_sleep(PS1); SB(); }
        asm volatile("s_waitcnt vmcnt(16)" ::: "memory");
        RAWBAR();
#pragma unroll
        for (int i = 0; i < 7; ++i) { dma(rs, l16, gu + (140 + i) * 1024, lds0 + L_GU5 + (kq * 7 + i) * 1024); if (DSL) __builtin_amdgcn_s_sleep(DSL); }
#pragma unroll
        for (int i = 0; i < 7; ++i) { dma(rs, l16, gu + (168 + i) * 1024, lds0 + L_GU6 + (kq * 7 + i) * 1024); if (DSL) __builtin_amdgcn_s_sleep(DSL); }
        asm volatile("s_waitcnt vmcnt(7)" ::: "memory");
        RAWBAR();
#pragma unroll
        for (int i = 0; i < 8; ++i) { dma(rs, l16, wdo + ((i & 1) * 112 + 7 * (((i >> 1) + j8) & 3) + 4) * 1024, lds0 + L_WD1 + (kq * 8 + i) * 1024); if (DSL) __builtin_amdgcn_s_sleep(DSL); }
        asm volatile("s_waitcnt vmcnt(8)" ::: "memory");
        RAWBAR();
        STAMP_AT(8 + 16 * lay);
        {
            const u32 ho = o_hf + (2 * g * 112 + 28 * kq) * 1024;
            u32 hs[4][2], l16b = l16 + 4096; PIN(l16b);
#pragma unroll
            for (int i = 0; i < 8; ++i) { hs[i >> 1][i & 1] = ho + ((i & 1) * 112 + 7 * (((i >> 1) + j8) & 3)) * 1024; asm volatile("" : "+s"(hs[i >> 1][i & 1])); }
            auto hld = [&](int s, int n) { return SAMEH ? ldx(rs, l16, hs[s & 3][n]) : ldx(rs, (s < 16 ? l16 : l16b) + ((s >> 2) & 3) * 1024, hs[s & 3][n]); };
            u32x4 hb[28][2], al[2][2];
            auto window = [&](int s0, int s1, u64 bad) {
                if (__builtin_expect(bad != 0, 0)) {
                    for (int sp = 0; !wt.dead; ++sp) {
                        asm volatile("" ::: "memory");
#pragma unroll
                        for (int z = s0; z < s1 && z < 28; ++z) { hb[z][0] = hld(z, 0); hb[z][1] = hld(z, 1); }
                        __builtin_amdgcn_s_waitcnt(0x0F70);
                        if (all_written<lay>(fold_all<lay, 2>(hb[s0]))) break;
                        if (sp > SPIN_CAP) wt.dead = true;
                    }
                }
            };
#pragma unroll
            for (int s = 0; s < 5; ++s) { hb[s][0] = hld(s, 0); hb[s][1] = hld(s, 1); }
            SB();
#pragma unroll
            for (int i = 0; i < 16; ++i) lds_st(lds, L_WD2 + (kq * 16 + i) * 1024 + l16, P3[i]);
            SB();
            hb[5][0] = hld(5, 0); hb[5][1] = hld(5, 1);
            u32 lw1 = L_WD1 + kq * 8192 + l16, lw2 = L_WD2 + kq * 16384 + l16; PIN(lw1); PIN(lw2);
            f32x16 acc[4]; const f32x16 zero = {};
            window(0, 6, unwritten<lay>(fold_all<lay, 2>(hb[0])));
            STAMP_AT(9 + 16 * lay);
#pragma unroll
            for (int s = 0; s < 28; ++s) {
                const u32x4 a0 = s < 8 ? P[(2 * s) & 15] : s < 16 ? P2[(2 * s) & 15] : al[s & 1][0], a1 = s < 8 ? P[(2 * s + 1) & 15] : s < 16 ? P2[(2 * s + 1) & 15] : al[s & 1][1];
                const bool two = s >= 1 && s < 1 + DIST - 6;
                const int z = s < 1 ? 6 : two ? 5 + 2 * s : s + DIST;
                acc[0] = mfma(a0, hb[s][0], s == 0 ? zero : acc[0]);
                if (z < 28) hb[z][0] = hld(z, 0);
                if (two) hb[z][1] = hld(z, 1);
                SB();
                acc[2] = mfma(a1, hb[s][0], s == 0 ? zero : acc[2]);
                if (two) { hb[z + 1][0] = hld(z + 1, 0); hb[z + 1][1] = hld(z + 1, 1); } else if (z < 28) hb[z][1] = hld(z, 1);
                if (s + 1 >= 16 && s + 1 < 28) {
                    const u32 lo = s + 1 < 20 ? lw1 + 2 * (s + 1 - 16) * 1024 : lw2 + 2 * (s + 1 - 20) * 1024;
                    al[(s + 1) & 1][0] = lds_ld(lds, lo); al[(s + 1) & 1][1] = lds_ld(lds, lo + 1024);
                }
                SB();
                acc[1] = mfma(a0, hb[s][1], s == 0 ? zero : acc[1]);
                u64 bad = 0;
                if (s + 1 < 28) bad = unwritten<lay>(fold_all<lay, 2>(hb[s + 1]));
                SB();
                acc[3] = mfma(a1, hb[s][1], s == 0 ? zero : acc[3]);
                SB();
                if (s + 1 < 28) window(s + 1, two ? z + 2 : z + 1, bad);
                SB();
            }
            STAMP_AT(10 + 16 * lay);
#pragma unroll
            for (int c = 0; c < 8; ++c) lds_st(lds, L_WD1 + (kq * 8 + c) * 1024 + l16, pack8(acc[c >> 1], c & 1));
        }
        BAR();
        STAMP_AT(11 + 16 * lay);
        if (lay == 0) {
#pragma unroll
        for (int i = 0; i < 14; ++i) dma(rs, l16, OFF_WQ + (j8 * 56 + kq * 14 + i) * 1024, lds0 + L_WQ + (kq * 14 + i) * 1024);
        if (j8 < 4) {
#pragma unroll
            for (int i = 0; i < 7; ++i) dma(rs, l16, OFF_WKV + ((j8 >> 1) * 28 + kq * 7 + i) * 1024, lds0 + L_WKV + (kq * 7 + i) * 1024);
        }
        }
    }
    if (lay == 0) {
        FRESH_LANE();
        const int kq = w >> 1;
        const bool kvjob = j8 < 4 && nn == (j8 & 1), isv = j8 >> 1;
        const u32 xo = o_x2 + (ntile * 28 + 7 * kq) * 1024;
        const int sn = w < 4 ? ntile : 2 * g + (j8 & 1);
        const u32 so = o_st2 + sn * 4096, sv = (4 * h) * 512 + m * 16;
        const bool fin = w < 4 || (w == 4 && j8 < 4);
        const int kind = w < 4 ? 0 : (isv ? 2 : 1), tile = w < 4 ? 2 * j8 + mt : 16 + isv;
        u32x4 x[7], sr[4], wa[21]; f32x4 cs[4];
        asm volatile("s_barrier" ::: "memory");
#pragma unroll
        for (int i = 0; i < 7; ++i) x[i] = ldx(rs, l16, xo + i * 1024);
        asm volatile("s_waitcnt vmcnt(7)\n\ts_barrier" ::: "memory");
        if (fin) {
#pragma unroll
            for (int i = 0; i < 4; ++i) sr[i] = ldx(rs, sv, so + i * 512);
            load_cs(cs, kind, rs, tile, lane);
        }
#pragma unroll
        for (int i = 0; i < 7; ++i) { wa[i] = lds_ld(lds, L_WQ + (7 * kq + i) * 1024 + l16); wa[7 + i] = lds_ld(lds, L_WQ + (28 + 7 * kq + i) * 1024 + l16); }
        if (kvjob) {
#pragma unroll
            for (int i = 0; i < 7; ++i) wa[14 + i] = lds_ld(lds, L_WKV + (7 * kq + i) * 1024 + l16);
        }
        const Sel sel3 = make_sel(l16 >> 4);
        u32 lpe = L_PE + l16; PIN(lpe);
        SB();
        settle<1, 7>(x, unwritten<1>(fold_all<1, 7>(x)), wt, [&](int i) { return ldx(rs, l16, xo + i * 1024); });
        STAMP_AT(13 + 16 * lay);
        const f32x16 zero = {};
        f32x16 qa[2], ka;
        if (kvjob) {
            if (isv) {
#pragma unroll
                for (int i = 0; i < 7; ++i) ka = mfma(x[i], wa[14 + i], i ? ka : zero);
            } else {
#pragma unroll
                for (int i = 0; i < 7; ++i) ka = mfma(wa[14 + i], x[i], i ? ka : zero);
            }
        }
#pragma unroll
        for (int i = 0; i < 7; ++i) qa[0] = mfma(wa[i], x[i], i ? qa[0] : zero);
        if (kvjob) { lds_st(lds, lpe + (kq * 10 + 8) * 1024, pack8(ka, 0)); lds_st(lds, lpe + (kq * 10 + 9) * 1024, pack8(ka, 1)); }
#pragma unroll
        for (int i = 0; i < 7; ++i) qa[1] = mfma(wa[7 + i], x[i], i ? qa[1] : zero);
        float mean = 0.f, rstd = 0.f;
        if (fin) {
            settle<2, 4>(sr, unwritten<2>(fold_all<2, 4>(sr)), wt, [&](int i) { return ldx(rs, sv, so + i * 512); });
            stats_from(sr, mean, rstd);
        }
        PIN(mean); PIN(rstd);
#pragma unroll
        for (int c = 0; c < 4; ++c) lds_st(lds, lpe + (kq * 10 + (c >> 1) * 4 + nn * 2 + (c & 1)) * 1024, pack8(qa[c >> 1], c & 1));
        f32x16 y = {}; PIN(y);
        BAR();
        STAMP_AT(14 + 16 * lay);
        if (fin) {
        y = add_tiles<4>(sel3, lds, lpe + 2 * w * 1024, 10240, y);
        const u32 dst = w < 4 ? w * 2048 : (u32)OFF_KV2 + (sn * 4 + (isv ? 2 : 0)) * 1024;
        qkv_finish<false>(y, kind, mean, rstd, cs, rs, lds, dst, (float*)(smem + L_SC), lane);
        STAMP_AT(15 + 16 * lay);
        if (w == 4) VM0();
        }
        if (w == 4 && j8 >= 4) __builtin_amdgcn_s_sleep(KVSL);
        RAWBAR();
    }
    STAMP_DUMP();
}

__global__ void __launch_bounds__(512) k_main(char* ws, const uint16_t* xin, uint16_t* out) {
    extern __shared__ __attribute__((aligned(16))) char smem[];
    Wait wt;
    layer<0>(ws, xin, out, smem, wt);
    layer<1>(ws, xin, out, smem, wt);
}

extern "C" void kernel_launch(void* const* d_in, const int* in_sizes, int n_in, void* d_out, int out_size, void* d_ws, size_t ws_size, hipStream_t stream) {
    const uint16_t* x = (const uint16_t*)d_in[0];
    char* ws = (char*)d_ws;
    (void)hipFuncSetAttribute((const void*)k_main, hipFuncAttributeMaxDynamicSharedMemorySize, L_TOTAL);
    (void)hipFuncSetAttribute((const void*)k_first, hipFuncAttributeMaxDynamicSharedMemorySize, F_TOTAL);
    k_first<<<256, 768, F_TOTAL, stream>>>(x, (const uint16_t*)d_in[1], (const uint16_t*)d_in[2], (const uint16_t*)d_in[3], (const uint16_t*)d_in[4],
                                               (const uint16_t*)d_in[6], (const uint16_t*)d_in[7], (const uint16_t*)d_in[8], ws);
    k_main<<<256, 512, L_TOTAL, stream>>>(ws, x, (uint16_t*)d_out);
}
```

```cpp
#include <hip/hip_runtime.h>
#include <hip/hip_bf16.h>
#include <stdint.h>
#include <type_traits>

#define FLIP 20
#define TOUCH 1
#define T45 8
#define T6 18
#define DIST 6
#define SAMEH 0
#define P3AT 4
#define PS0 19
#define PS1 2
#define RING 4
#define DSL 0
#define NOHST 0
#define CODEPF 1
#define MAP 2
#define KVSL 34
#define ARMAT 0
#define RSL 30
#define FLIP0 16
#define ARMK 0
#define TWOST 0
#define NA 8
#define NB 8
#define NC 0
#define ND 0
#define HF0 7
#define RWAIT 2
#define FLIP1 0
#define CODEKB 80
#define U2 14
typedef unsigned u32;
typedef u32 u32x2 __attribute__((ext_vector_type(2)));
typedef u32 u32x4 __attribute__((ext_vector_type(4)));
typedef float f32x2 __attribute__((ext_vector_type(2)));
typedef float f32x4 __attribute__((ext_vector_type(4)));
typedef float f32x8 __attribute__((ext_vector_type(8)));
typedef float f32x16 __attribute__((ext_vector_type(16)));
typedef __bf16 bf16x2 __attribute__((ext_vector_type(2)));
typedef __bf16 bf16x8 __attribute__((ext_vector_type(8)));
typedef __bf16 bf16x16 __attribute__((ext_vector_type(16)));
#define DI __device__ __forceinline__

constexpr int S_ = 2048, D_ = 448, F_ = 1792, DH = 28;
constexpr size_t KB = 1024;
constexpr size_t OFF_WQ = 0;
constexpr size_t OFF_WKV = OFF_WQ + 448 * KB;
constexpr size_t OFF_WO = OFF_WKV + 56 * KB;
constexpr size_t OFF_WGU = OFF_WO + 512 * KB;
constexpr size_t OFF_WD = OFF_WGU + 3136 * KB;
constexpr size_t OFF_CS = OFF_WD + 1792 * KB;
constexpr size_t OFF_CG = OFF_CS + 4 * KB;
constexpr size_t OFF_KV = OFF_CG + 56 * KB;
constexpr size_t OFF_F1 = OFF_KV + 256 * KB;
constexpr size_t OFF_F2 = OFF_F1 + 16 * KB;
constexpr size_t OFF_EP = OFF_F2 + 8 * KB;
constexpr size_t OFF_HO = OFF_EP + 1 * KB;
constexpr size_t OFF_OF = OFF_HO;
constexpr size_t OFF_X1 = OFF_OF + 2048 * KB;
constexpr size_t OFF_X2 = OFF_X1 + 1792 * KB;
constexpr size_t OFF_S1 = OFF_X2 + 1792 * KB;
constexpr size_t OFF_S2 = OFF_S1 + 256 * KB;
constexpr size_t OFF_KV2 = OFF_S2 + 256 * KB;
constexpr size_t OFF_HF = OFF_KV2 + 256 * KB;
constexpr size_t WS_END = OFF_HF + 7168 * KB;
constexpr int HO_KB = (int)((OFF_HF - OFF_HO) / KB);

#define STAMP_AT(i)
#define STAMP_INIT()
#define STAMP_DUMP()
DI f32x16 mfma(u32x4 a, u32x4 b, f32x16 c) {
    return __builtin_amdgcn_mfma_f32_32x32x16_bf16(__builtin_bit_cast(bf16x8, a), __builtin_bit_cast(bf16x8, b), c, 0, 0, 0);
}
DI f32x16 smfmac_hi(u32x4 a, f32x8 b, f32x16 c) {
    return __builtin_amdgcn_smfmac_f32_32x32x32_bf16(__builtin_bit_cast(bf16x8, a), __builtin_bit_cast(bf16x16, b), c, 0xDDDD, 0, 0);
}
DI u32 pk(float lo, float hi) { f32x2 v = {lo, hi}; return __builtin_bit_cast(u32, __builtin_convertvector(v, bf16x2)); }
DI float dot2(u32 a, u32 b, float c) { return __builtin_amdgcn_fdot2_f32_bf16(__builtin_bit_cast(bf16x2, a), __builtin_bit_cast(bf16x2, b), c, false); }
DI float blo(u32 x) { return __uint_as_float(x << 16); }
DI float bhi(u32 x) { return __uint_as_float(x & 0xffff0000u); }
DI float xhalf_sum(float v) { auto r = __builtin_amdgcn_permlane32_swap(__float_as_uint(v), __float_as_uint(v), false, false); return __uint_as_float(r[0]) + __uint_as_float(r[1]); }
DI float xhalf_max(float v) { auto r = __builtin_amdgcn_permlane32_swap(__float_as_uint(v), __float_as_uint(v), false, false); return fmaxf(__uint_as_float(r[0]), __uint_as_float(r[1])); }
DI u32x4 pack8(const f32x16& x, int s) { return (u32x4){pk(x[8 * s], x[8 * s + 1]), pk(x[8 * s + 2], x[8 * s + 3]), pk(x[8 * s + 4], x[8 * s + 5]), pk(x[8 * s + 6], x[8 * s + 7])}; }
DI int prow(int h, int j) { return 8 * (j >> 2) + 4 * h + (j & 3); }
DI int feat(int ks, int h, int j) {
    const int p = ks / 7, s = ks % 7;
    if (s < 6) return 112 * p + (s >= 3 ? 56 + 16 * (s - 3) : 16 * s) + prow(h, j);
    return 112 * p + 56 * (j >> 2) + 48 + 4 * h + (j & 3);
}

DI void ln_stats(float s1, float s2, float& mean, float& rstd) {
    s1 = xhalf_sum(s1); s2 = xhalf_sum(s2);
    mean = s1 * (1.f / D_);
    const float var = fmaxf(s2 * (1.f / D_) - mean * mean, 0.f);
    rstd = __builtin_amdgcn_rsqf(var + 1e-5f);
}

typedef __amdgpu_buffer_rsrc_t rsrc_t;
typedef __attribute__((address_space(3))) char* lds_t;
constexpr u32 OOB = 0x80000000u;
constexpr u32 ARMED = 0xFFFFFFFFu;
DI u32x4 ldx(rsrc_t r, u32 v, u32 s) { return __builtin_amdgcn_raw_buffer_load_b128(r, v, s, 16); }
#define HOLD(x) asm volatile("s_nop 1" ::"v"(x))
DI void st(u32x4 x, rsrc_t r, u32 v, u32 s) { __builtin_amdgcn_raw_buffer_store_b128(x, r, v, s, 0); HOLD(x); }
DI void stx(u32x4 x, rsrc_t r, u32 v, u32 s) { __builtin_amdgcn_raw_buffer_store_b128(x, r, v, s, 16); HOLD(x); }
DI void stx2(u32x2 x, rsrc_t r, u32 v, u32 s) { __builtin_amdgcn_raw_buffer_store_b64(x, r, v, s, 16); }
DI void dma(rsrc_t r, u32 v, u32 s, u32 l) { asm volatile("s_mov_b32 m0, %0\n\ts_nop 0\n\tbuffer_load_dwordx4 %1, %2, %3 offen sc1 lds" ::"s"(l), "v"(v), "s"(r), "s"(s) : "m0", "memory"); }
DI u32x4 lds_ld(lds_t p, u32 off) { return *(__attribute__((address_space(3))) u32x4*)(p + off); }
DI void lds_st(lds_t p, u32 off, u32x4 v) { *(__attribute__((address_space(3))) u32x4*)(p + off) = v; }
DI u32 umax(u32 a, u32 b) { return a > b ? a : b; }
DI u32 umin(u32 a, u32 b) { return a < b ? a : b; }
template <int MODE> DI u32 mark1(u32 a) { return MODE == 0 ? (a & ~1u) : MODE == 1 ? (a | 1u) : umin(a, ARMED - 1); }
template <int MODE> DI u32x4 mark(u32x4 a) { return (u32x4){mark1<MODE>(a[0]), mark1<MODE>(a[1]), mark1<MODE>(a[2]), mark1<MODE>(a[3])}; }
template <int MODE> DI u32 fold(u32 m, u32x4 a) {
    return MODE == 0 ? ((m | a[0] | a[1]) | a[2] | a[3]) : MODE == 1 ? ((m & a[0] & a[1]) & a[2] & a[3]) : umax(umax(umax(m, a[0]), a[1]), umax(a[2], a[3]));
}
template <int MODE, int N> DI u32 fold_all(const u32x4* x) {
    u32 m = MODE == 1 ? ~0u : 0u;
#pragma unroll
    for (int i = 0; i < N; ++i) m = fold<MODE>(m, x[i]);
    return m;
}
template <int MODE> DI bool all_written(u32 m) { return __all(MODE == 0 ? (m & 1u) == 0u : MODE == 1 ? (m & 1u) != 0u : m != ARMED); }
typedef unsigned long long u64;
template <int MODE> DI u64 unwritten(u32 m) { u64 b = __ballot(MODE == 0 ? (m & 1u) != 0u : MODE == 1 ? (m & 1u) == 0u : m == ARMED); asm volatile("" : "+s"(b)); return b; }
#define SB() __builtin_amdgcn_sched_barrier(0)
#define RAWBAR() asm volatile("s_barrier" ::: "memory")
#define PIN(x) asm volatile("" : "+v"(x))
#define BAR() asm volatile("s_waitcnt lgkmcnt(0)\n\ts_barrier" ::: "memory")
#define VM0() asm volatile("s_waitcnt vmcnt(0)" ::: "memory")
constexpr int SPIN_CAP = 1 << 15;
#define COUNT_RETRY()
struct Wait { bool dead = false; int retries = 0; };
template <int MODE, int N, class F> DI void settle(u32x4* x, u64 bad, Wait& wt, F&& reload) {
    if (__builtin_expect(bad != 0, 0)) {
        for (int sp = 0; !wt.dead; ++sp) {
            __builtin_amdgcn_s_sleep(1);
            asm volatile("" ::: "memory");
#pragma unroll
            for (int i = 0; i < N; ++i) x[i] = reload(i);
            if (all_written<MODE>(fold_all<MODE, N>(x))) break;
            if (sp > SPIN_CAP) wt.dead = true;
        }
    }
}
template <int MODE, class F> DI void gated(rsrc_t rs, u32 pv, u32 ps, Wait& wt, F&& batch) {
    for (int sp = 0;; ++sp) {
        asm volatile("" ::: "memory");
        const u32 p = __builtin_amdgcn_raw_buffer_load_b32(rs, pv, ps, 16);
        batch();
        asm volatile("" ::: "memory");
        if (__builtin_expect(all_written<MODE>(p) || wt.dead, 1)) break;
        if (sp > SPIN_CAP) wt.dead = true;
    }
}
DI u32 wave_umax(u32 v) {
    v = umax(v, __builtin_amdgcn_update_dpp(0u, v, 0x111, 0xf, 0xf, false));
    v = umax(v, __builtin_amdgcn_update_dpp(0u, v, 0x112, 0xf, 0xf, false));
    v = umax(v, __builtin_amdgcn_update_dpp(0u, v, 0x114, 0xf, 0xf, false));
    v = umax(v, __builtin_amdgcn_update_dpp(0u, v, 0x118, 0xf, 0xf, false));
    v = umax(v, __builtin_amdgcn_update_dpp(0u, v, 0x142, 0xa, 0xf, false));
    v = umax(v, __builtin_amdgcn_update_dpp(0u, v, 0x143, 0xc, 0xf, false));
    return __builtin_amdgcn_readlane(v, 63);
}

DI void load_cs(f32x4* c, int kind, rsrc_t rs, int tile, int lane) {
    if (kind != 2) {
#pragma unroll
        for (int i = 0; i < 4; ++i) c[i] = __builtin_bit_cast(f32x4, ldx(rs, (lane >> 5) * 16, OFF_CS + tile * 128 + i * 32));
    } else c[0][0] = __uint_as_float(__builtin_amdgcn_raw_buffer_load_b32(rs, (lane & 31) * 4, OFF_CS + tile * 128, 16));
}
template <bool NORMED> DI u32 qkv_finish(f32x16 acc, int kind, float mean, float rstd, const f32x4* cs, rsrc_t rs, lds_t lds, u32 dst, float* sc, int lane) {
    const int m = lane & 31, h = lane >> 5;
    u32 kb = 0;
    if (kind != 2) {
        const float s = kind == 0 ? rstd * (0.18898223650461362f * 1.4426950408889634f) : rstd;
#pragma unroll
        for (int r = 0; r < 16; ++r) acc[r] = NORMED ? acc[r] * s : (acc[r] - mean * cs[r >> 2][r & 3]) * s;
        u32x4 k0 = pack8(acc, 0), k1 = pack8(acc, 1);
        float nn = 0.f, n2 = 0.f;
#pragma unroll
        for (int i = 0; i < 4; ++i) { nn = dot2(k0[i], k0[i], nn); n2 = dot2(k1[i], k1[i], n2); }
        nn = xhalf_sum(nn + n2);
        if (kind == 0) {
            k1[2] = h ? pk(__builtin_amdgcn_sqrtf(nn) * 1.01f, 0.f) : k1[2];
            lds_st(lds, dst + lane * 16, k0); lds_st(lds, dst + lane * 16 + 1024, k1);
        } else {
            kb = pk(__builtin_amdgcn_sqrtf(__uint_as_float(wave_umax(__float_as_uint(nn)))) * 1.01f, 0.f);
            k1[2] = h ? 0x3f803f80u : k1[2];
            k1[3] = h ? kb : k1[3];
            const int slot = (m & 19) | ((m & 4) << 1) | ((m & 8) >> 1);
            stx(mark<2>(k0), rs, (slot + 32 * h) * 16, dst); stx(mark<2>(k1), rs, (slot + 32 * h) * 16, dst + 1024);
        }
    } else {
        sc[lane] = h ? rstd : mean;
        __builtin_amdgcn_fence(__ATOMIC_RELEASE, "wavefront"); __builtin_amdgcn_wave_barrier(); __builtin_amdgcn_fence(__ATOMIC_ACQUIRE, "wavefront");
#pragma unroll
        for (int i = 0; i < 4; ++i) {
            const f32x4 mu = *(const f32x4*)(sc + 8 * i + 4 * h), rr = *(const f32x4*)(sc + 32 + 8 * i + 4 * h);
#pragma unroll
            for (int r = 0; r < 4; ++r) acc[4 * i + r] = m < 28 ? (NORMED ? acc[4 * i + r] : acc[4 * i + r] - mu[r] * cs[0][0]) * rr[r] : (m == 28 ? 1.f : 0.f);
        }
        stx(mark<2>(pack8(acc, 0)), rs, lane * 16, dst); stx(mark<2>(pack8(acc, 1)), rs, lane * 16, dst + 1024);
    }
    return kb;
}

DI void stats_from(const u32x4* s, float& mean, float& rstd) {
    float s1 = 0.f, s2 = 0.f;
#pragma unroll
    for (int i = 0; i < 4; ++i) { s1 += __uint_as_float(s[i][0]) + __uint_as_float(s[i][2]); s2 += __uint_as_float(s[i][1]) + __uint_as_float(s[i][3]); }
    ln_stats(s1, s2, mean, rstd);
}

struct Sel { u32x4 a; u32 idx; };
DI Sel make_sel(int lane) {
    asm volatile("" : "+v"(lane));
    const int m = lane & 31, grp = ((m >> 1) & 2) | ((m >> 3) & 1);
    const u32 one = (m >> 4) == (lane >> 5) ? 0x3f80u : 0u;
    Sel s; s.idx = (m & 3) * 0x5555u;
#pragma unroll
    for (int i = 0; i < 4; ++i) s.a[i] = grp == i ? one : 0u;
    PIN(s.a); PIN(s.idx);
    return s;
}
DI f32x16 add_tile(const Sel& s, u32x4 lo, u32x4 hi, f32x16 c) {
    return __builtin_amdgcn_smfmac_f32_32x32x32_bf16(__builtin_bit_cast(bf16x8, s.a), __builtin_bit_cast(bf16x16, __builtin_shufflevector(lo, hi, 0, 1, 2, 3, 4, 5, 6, 7)), c, s.idx, 0, 0);
}
template <int N> DI f32x16 add_tiles(const Sel& s, lds_t lds, u32 off, u32 stride, f32x16 acc) {
    u32x4 t[N][2];
#pragma unroll
    for (int u = 0; u < N; ++u) { t[u][0] = lds_ld(lds, off + u * stride); t[u][1] = lds_ld(lds, off + u * stride + 1024); }
    SB();
#pragma unroll
    for (int u = 0; u < N; ++u) acc = add_tile(s, t[u][0], t[u][1], acc);
    return acc;
}

typedef short s16x4 __attribute__((ext_vector_type(4)));
DI u32x4 tr8(lds_t lds, u32 pa, u32 pb) {
    const u32x2 lo = __builtin_bit_cast(u32x2, __builtin_amdgcn_ds_read_tr16_b64_v4i16((__attribute__((address_space(3))) s16x4*)(lds + pa)));
    const u32x2 hi = __builtin_bit_cast(u32x2, __builtin_amdgcn_ds_read_tr16_b64_v4i16((__attribute__((address_space(3))) s16x4*)(lds + pb)));
    return (u32x4){lo[0], lo[1], hi[0], hi[1]};
}
constexpr u32 F_X = 0, F_WQ = 57344, F_WK = F_WQ + 50432, F_JOB = F_WK + 25600, F_Q = 40 * 1024, F_ST = 48 * 1024, F_SC = 50 * 1024, F_KN = 160 * 1024 - 640, F_TOTAL = 160 * 1024;
constexpr u32 F_UP = 14336 + 64;
constexpr u32 F_CNT = F_TOTAL - 128;
static_assert(F_JOB + F_UP + 14336 + 1024 <= F_KN, "LDS");
DI rsrc_t srd(const void* p, int bytes) { return __builtin_amdgcn_make_buffer_rsrc((void*)p, 0, bytes, 0x00020000); }
DI int fblk(int s, int half) { return s < 6 ? (s >= 3 ? 56 + 16 * (s - 3) : 16 * s) + 8 * half : 56 * half + 48; }
typedef __attribute__((address_space(3))) u32* ldsw_t;
DI u32 arrive(lds_t lds, u32 off, int lane) {
    asm volatile("s_waitcnt lgkmcnt(0)" ::: "memory");
    const u32 old = __hip_atomic_fetch_add((ldsw_t)(lds + off + (lane ? 64 : 0)), 1u, __ATOMIC_RELAXED, __HIP_MEMORY_SCOPE_WORKGROUP);
    asm volatile("" ::: "memory");
    return __builtin_amdgcn_readfirstlane(old);
}
DI void wait_for(lds_t lds, u32 off, u32 n) {
    while (__builtin_amdgcn_readfirstlane(__hip_atomic_load((ldsw_t)(lds + off), __ATOMIC_RELAXED, __HIP_MEMORY_SCOPE_WORKGROUP)) < n) __builtin_amdgcn_s_sleep(0);
    asm volatile("" ::: "memory");
}
struct In { const uint16_t *x, *wq, *wk, *wv, *wo, *wg, *wu, *wd; };

constexpr u32 L_RED = 0, L_GU0 = 64 * 1024, L_GU3 = 32 * 1024, L_GU4 = 0, L_GU5 = 64 * 1024, L_GU6 = 92 * 1024, L_WD1 = 120 * 1024, L_WD2 = 0,
              L_CS = 152 * 1024, L_SC = 154 * 1024, L_KN = 155 * 1024, L_WQ = 0, L_WKV = 56 * 1024, L_PE = 84 * 1024, L_TOTAL = 160 * 1024;

template <int lay> DI void layer(char* ws, const In& in, uint16_t* out, char* smem, Wait& wt, u32 token) {
    const lds_t lds = (lds_t)smem;
    const u32 lds0 = (u32)(uintptr_t)lds;
    int b = blockIdx.x, w = __builtin_amdgcn_readfirstlane(threadIdx.x >> 6);
    asm volatile("" : "+s"(b), "+s"(w));
    const int g = (b & 7) * 4 + (b >> 6), j8 = (b >> 3) & 7;
    const int blk1 = MAP == 2 ? (b & 1) * 8 + ((b >> 3) & 7) : MAP == 1 ? ((b >> 1) & 3) * 4 + ((b >> 3) & 3) : g >> 1;
    const int i16 = MAP == 2 ? ((b >> 1) & 3) * 4 + (b >> 6) : MAP == 1 ? (b & 1) * 8 + (b >> 5) : (b >> 3) & 15;
    u32 l16 = __builtin_amdgcn_mbcnt_hi(~0u, __builtin_amdgcn_mbcnt_lo(~0u, 0u)) * 16; PIN(l16);
#define FRESH_LANE() u32 l16_ = l16; PIN(l16_); const int lane = l16_ >> 4, m = lane & 31, h = lane >> 5; (void)m; (void)h
    const rsrc_t rs = __builtin_amdgcn_make_buffer_rsrc(ws, 0, (int)WS_END, 0x00020000);
    constexpr u32 o_x1 = OFF_X1, o_x2 = OFF_X2, o_st1 = OFF_S1, o_st2 = OFF_S2, o_hf = OFF_HF;
    const int mt = (w >> 1) & 1, nn = w & 1, ntile = 2 * g + nn;
    const int ks0 = 7 * (j8 >> 1) + 3 * (j8 & 1), ksh = 7 * (j8 >> 1) + 6;
    const u32 xo0 = (ntile * 28 + ks0 + 2 * mt) * 1024, xo1 = mt ? (ntile * 28 + ksh) * 1024 + 8 * (j8 & 1) : xo0 + 1024;
    const u32 sto = ntile * 4096 + j8 * 512 + mt * 8;
    STAMP_INIT();
    STAMP_AT(0 + 16 * lay);

    const bool isg = b < 112, isd = !isg && b < 168;
    const int jb = isg ? b : isd ? b - 112 : b - 168;
    auto publish = [&](int lane) {
        const u32 old = arrive(lds, F_CNT + 16, lane);
        __builtin_amdgcn_raw_buffer_store_b32(token, rs, (lane || old != 7u) ? OOB : 0u, OFF_F2 + b * 32, 16);
    };
    auto wq_piece = [&](rsrc_t r, u32 v, int e) { int p = e + 12 * (g & 3); p = p >= 50 ? p - 50 : p; dma(r, v, p * 8064 + 112 * j8, lds0 + F_WQ + p * 1008); };
    auto start_a = [&]() {
        FRESH_LANE();
        const int kq = w, q4 = (lane & 15) >> 2;
        const u32 c8 = ((lane >> 4) & 1) * 32 + (lane & 3) * 8;
        const bool kvwg = j8 < 4, isv = j8 >> 1;
        const rsrc_t rx = srd(in.x, S_ * D_ * 2), rq_ = srd(in.wq, D_ * D_ * 2), rk_ = srd(isv ? in.wv : in.wk, D_ * DH * 2);
        const u32 xv = (lane >> 3) * 128 + (((lane & 7) ^ (lane >> 4)) * 16), wvo = (lane / 7) * 896 + (lane % 7) * 16;
        auto xpiece = [&](int e) { int p = e + 7 * j8; p = p >= 56 ? p - 56 : p; dma(rx, xv ^ ((p & 1) * 64), g * 57344 + p * 1024, lds0 + F_X + p * 1024); };
        auto kpiece = [&](int e) { int p = e + 3 * (2 * (g & 3) + (j8 & 1)); p = p >= 25 ? p - 25 : p; dma(rk_, l16, p * 1024, lds0 + F_WK + p * 1024); };
        xpiece(w); xpiece(4 + w);
        if (kvwg) kpiece(w);
        if (l16 < 512 && w == 0) *(u32*)(smem + F_CNT + (l16 >> 2)) = 0u;
        BAR();
#pragma unroll
        for (int i = 2; i < 14; ++i) xpiece(4 * i + w);
        if (kvwg) {
#pragma unroll
            for (int i = 1; i < 6; ++i) kpiece(4 * i + w);
            if (w == 0) kpiece(24);
        }
#pragma unroll
        for (int i = 3; i < 12; ++i) wq_piece(rq_, wvo, 4 * i + w);
        wq_piece(rq_, wvo, 48 + (w & 1));
        u32 xa[7], rq = F_WQ + (112 * kq + 8 * h + q4) * 112 + c8, rk = F_WK + (112 * kq + 8 * h + q4) * 56 + c8;
#pragma unroll
        for (int i = 0; i < 7; ++i) { const int ch = 14 * kq + 2 * i + h, ln = 7 * m + (ch >> 3); xa[i] = F_X + ln * 128 + 16 * ((ch ^ (ln >> 1)) & 7); PIN(xa[i]); }
        PIN(rq); PIN(rk);
        const Sel sel = make_sel(lane);
        asm volatile("s_waitcnt vmcnt(10)" ::: "memory");
        arrive(lds, F_CNT, lane); wait_for(lds, F_CNT, 4u);
        STAMP_AT(14 + 16);
        u32x4 xf[2][7], aq[2][7], ak[7];
        if (kvwg) {
#pragma unroll
            for (int i = 0; i < 7; ++i) ak[i] = tr8(lds, rk + i * 896, rk + i * 896 + 224);
        }
#pragma unroll
        for (int i = 0; i < 7; ++i) { xf[0][i] = lds_ld(lds, xa[i]); xf[1][i] = lds_ld(lds, xa[i] + 28672); }
        arrive(lds, F_CNT + 4, lane);
        const f32x16 zero = {};
        const u32x4 z4 = {0u, 0u, 0u, 0u};
        float st_[4] = {0.f, 0.f, 0.f, 0.f}, cq[2] = {0.f, 0.f}, ck = 0.f, ck2 = 0.f;
        f32x16 ka = zero, qa[4];
        if (kvwg) {
            if (j8 == 0) { _Pragma("unroll") for (int i = 0; i < 7; ++i) ka = mfma(ak[i], xf[0][i], ka); }
            else if (j8 == 1) { _Pragma("unroll") for (int i = 0; i < 7; ++i) ka = mfma(ak[i], xf[1][i], ka); }
            else if (j8 == 2) { _Pragma("unroll") for (int i = 0; i < 7; ++i) ka = mfma(xf[0][i], ak[i], ka); }
            else { _Pragma("unroll") for (int i = 0; i < 7; ++i) ka = mfma(xf[1][i], ak[i], ka); }
#pragma unroll
            for (int i = 0; i < 7; ++i) { ck = dot2(ak[i][0], 0x3f803f80u, ck); ck2 = dot2(ak[i][1], 0x3f803f80u, ck2); ck = dot2(ak[i][2], 0x3f803f80u, ck); ck2 = dot2(ak[i][3], 0x3f803f80u, ck2); }
        }
#pragma unroll
        for (int i = 0; i < 7; ++i)
#pragma unroll
            for (int e = 0; e < 4; ++e) { st_[0] = dot2(xf[0][i][e], 0x3f803f80u, st_[0]); st_[1] = dot2(xf[0][i][e], xf[0][i][e], st_[1]);
                                          st_[2] = dot2(xf[1][i][e], 0x3f803f80u, st_[2]); st_[3] = dot2(xf[1][i][e], xf[1][i][e], st_[3]); }
#pragma unroll
        for (int r = 12; r < 16; ++r) ka[r] = (h && !isv) ? 0.f : ka[r];
        wait_for(lds, F_CNT + 4, 4u);
        lds_st(lds, (kq * 10 + 8) * 1024 + l16, pack8(ka, 0)); lds_st(lds, (kq * 10 + 9) * 1024 + l16, pack8(ka, 1));
#pragma unroll
        for (int i = 0; i < 4; ++i) st_[i] = xhalf_sum(st_[i]);
        *(f32x2*)(smem + F_ST + (h * 4 + kq) * 256 + m * 8) = (f32x2){h ? st_[2] : st_[0], h ? st_[3] : st_[1]};
        { const float c = xhalf_sum(ck + ck2); *(float*)(smem + F_SC + (8 + kq) * 128 + m * 4) = m < 28 ? c : 0.f; }
        arrive(lds, F_CNT + 8, lane);
        auto finish = [&](int chunk, int n, int ct, int kind, u32 dst) {
            float s1 = 0.f, s2 = 0.f;
#pragma unroll
            for (int u = 0; u < 4; ++u) { const f32x2 t = *(const f32x2*)(smem + F_ST + (n * 4 + u) * 256 + m * 8); s1 += t[0]; s2 += t[1]; }
            const float c0 = *(const float*)(smem + F_SC + (ct * 4 + 2 * h) * 128 + m * 4), c1 = *(const float*)(smem + F_SC + (ct * 4 + 2 * h + 1) * 128 + m * 4);
            f32x16 y = add_tiles<4>(sel, lds, chunk * 1024 + l16, 10240, zero);
            const float nm = s1 * (-1.f / D_), rstd = __builtin_amdgcn_rsqf(fmaxf(s2 * (1.f / D_) - nm * nm, 0.f) + 1e-5f);
            const u32 mh = pk(nm, 0.f), mm = mh | (pk(nm - blo(mh), 0.f) << 16), h0 = pk(c0, c0), h1 = pk(c1, c1);
            const u32x4 fm = {mm, mm, mm, mm}, fc = {h0, pk(c0 - blo(h0), c0 - blo(h0)), h1, pk(c1 - blo(h1), c1 - blo(h1))};
            y = kind == 2 ? mfma(fm, fc, y) : mfma(fc, fm, y);
            return qkv_finish<true>(y, kind, 0.f, rstd, nullptr, rs, lds, dst, (float*)(smem + F_SC + 1536), lane);
        };
        if (kvwg && w == 0) {
            wait_for(lds, F_CNT + 8, 4u);
            const u32 kb = finish(8, j8 & 1, 2, isv ? 2 : 1, OFF_KV + ((2 * g + (j8 & 1)) * 4 + (isv ? 2 : 0)) * 1024);
            asm volatile("s_waitcnt vmcnt(2)" ::: "memory");
            arrive(lds, F_CNT + 32, lane);
            VM0();
            stx2((u32x2){token, kb << 16}, rs, lane ? OOB : 0u, OFF_F1 + ((2 * g + (j8 & 1)) * 2 + isv) * 128);
            STAMP_AT(13 + 16);
        } else { VM0(); arrive(lds, F_CNT + 32, lane); }
        wait_for(lds, F_CNT + 32, 8u);
#pragma unroll
        for (int i = 0; i < 7; ++i) { aq[0][i] = tr8(lds, rq + i * 1792, rq + i * 1792 + 448); aq[1][i] = tr8(lds, rq + i * 1792 + 56, rq + i * 1792 + 504); }
        SB();
#pragma unroll
        for (int i = 0; i < 7; ++i)
#pragma unroll
            for (int c = 0; c < 4; ++c) qa[c] = mfma(aq[c >> 1][i], xf[c & 1][i], i ? qa[c] : zero);
#pragma unroll
        for (int i = 0; i < 7; ++i)
#pragma unroll
            for (int e = 0; e < 4; ++e) { cq[0] = dot2(aq[0][i][e], 0x3f803f80u, cq[0]); cq[1] = dot2(aq[1][i][e], 0x3f803f80u, cq[1]); }
#pragma unroll
        for (int c = 0; c < 4; ++c) {
#pragma unroll
            for (int r = 12; r < 16; ++r) qa[c][r] = h ? 0.f : qa[c][r];
            lds_st(lds, (kq * 10 + 2 * c) * 1024 + l16, pack8(qa[c], 0)); lds_st(lds, (kq * 10 + 2 * c + 1) * 1024 + l16, pack8(qa[c], 1));
        }
        { const float c0 = xhalf_sum(cq[0]), c1 = xhalf_sum(cq[1]); *(float*)(smem + F_SC + (h * 4 + kq) * 128 + m * 4) = m < 28 ? (h ? c1 : c0) : 0.f; }
        arrive(lds, F_CNT + 12, lane);
        if (g == 0) {
#pragma unroll
            for (int i = 0; i < 14; ++i) { const u32 p = rq - 448 * h + (i / 7) * 56; const u32x4 f = tr8(lds, p + fblk(i % 7, 0) * 112, p + fblk(i % 7, 1) * 112);
                stx(m < 28 ? f : z4, rs, l16, OFF_WQ + ((2 * j8 + i / 7) * 28 + 7 * kq + i % 7) * 1024); }
        }
        if (g == 1 && (j8 & 5) == 0) {
#pragma unroll
            for (int i = 0; i < 7; ++i) { const u32x4 f = tr8(lds, rk - 224 * h + fblk(i, 0) * 56, rk - 224 * h + fblk(i, 1) * 56);
                stx(m < 28 ? f : z4, rs, l16, OFF_WKV + ((j8 >> 1) * 28 + 7 * kq + i) * 1024); }
        }
        wait_for(lds, F_CNT + 12, 4u);
        if (w == 3 && (g == 0 || (g == 1 && (j8 & 5) == 0))) {
            const float* p = (const float*)(smem + F_SC) + (g == 0 ? h : 2) * 128 + m;
            __builtin_amdgcn_raw_buffer_store_b32(__float_as_uint((p[0] + p[32]) + (p[64] + p[96])), rs, (g == 0 || h == 0) ? lane * 4 : OOB, OFF_CS + (g == 0 ? 2 * j8 : 16 + (j8 >> 1)) * 128, 16);
        }
        finish(2 * w, w & 1, w >> 1, 0, F_Q + w * 2048);
        if (g < 2) VM0();
        publish(lane);
        STAMP_AT(15 + 16);
    };
    auto start_b = [&]() {
        FRESH_LANE();
        const int v = w - 4;
        { const rsrc_t rq_ = srd(in.wq, D_ * D_ * 2); const u32 wvo = (lane / 7) * 896 + (lane % 7) * 16;
#pragma unroll
          for (int i = 0; i < 3; ++i) wq_piece(rq_, wvo, 4 * i + v); }
        BAR();
        u32x4 a4 = {ARMED, ARMED, ARMED, ARMED}; PIN(a4);
        if (CODEPF) {
            u64 pc; asm volatile("s_getpc_b64 %0" : "=s"(pc));
            u32x4 c = *(const __attribute__((address_space(1))) u32x4*)((pc & ~15ull) + (((b >> 3) * 4 + v) % CODEKB) * 1024 + l16);
            PIN(c);
        }
        VM0();
        arrive(lds, F_CNT + 32, lane);
#pragma unroll
        for (int i = 0; i < 7; ++i) stx(a4, rs, (i < 6 || v == 0) ? l16 : OOB, OFF_HO + (b * 25 + 4 * i + v) * 1024);
#pragma unroll
        for (int i = 0; i < 7; ++i) stx(a4, rs, l16, OFF_HF + (b * 28 + 4 * i + v) * 1024);
        if (RWAIT) wait_for(lds, RWAIT == 1 ? F_CNT : F_CNT + 32, RWAIT == 1 ? 4u : 8u);
        if (isg) {
            const rsrc_t rg = srd(v < 2 ? in.wg : in.wu, D_ * F_ * 2);
            const u32 gv = (lane >> 3) * 3584 + (lane & 7) * 16;
#pragma unroll
            for (int i = 0; i < 7; ++i) { const int p = (v & 1) + 2 * i; dma(rg, gv, (112 * (jb & 3) + 8 * p) * 3584 + 128 * (jb >> 2), lds0 + F_JOB + (v >> 1) * F_UP + p * 1024); }
        } else if (b < 184) {
            const rsrc_t rd = srd(isd ? in.wd : in.wo, isd ? F_ * D_ * 2 : D_ * D_ * 2);
#pragma unroll
            for (int i = 0; i < 7; ++i) { const int p = v + 4 * i; if (p < (isd ? 28 : 25)) dma(rd, l16, jb * (isd ? 28672 : 25088) + p * 1024, lds0 + F_JOB + p * 1024); }
        }
        VM0();
        arrive(lds, F_CNT + 20, lane); wait_for(lds, F_CNT + 20, 4u);
        STAMP_AT(14 + 16);
        const int q4 = (lane & 15) >> 2;
        if (isg) {
            const u32 base = F_JOB + ((lane >> 4) & 1) * F_UP + v * 32 + (lane & 3) * 8 + (4 * h + q4) * 128;
            u32x4 f[7]; float cs = 0.f;
#pragma unroll
            for (int i = 0; i < 7; ++i) f[i] = tr8(lds, base + fblk(i, 0) * 128, base + fblk(i, 1) * 128);
#pragma unroll
            for (int i = 0; i < 7; ++i) { stx(f[i], rs, l16, OFF_WGU + (((jb & ~3) + v) * 28 + 7 * (jb & 3) + i) * 1024);
#pragma unroll
                for (int e = 0; e < 4; ++e) cs = dot2(f[i][e], 0x3f803f80u, cs); }
            __builtin_amdgcn_raw_buffer_store_b32(__float_as_uint(xhalf_sum(cs)), rs, h ? OOB : m * 4, OFF_CG + ((jb & 3) * 112 + (jb & ~3) + v) * 128, 16);
        } else if (b < 184) {
            u32x4 f[8];
#pragma unroll
            for (int i = 0; i < 8; ++i) {
                const u32 a0 = F_JOB + (16 * (i & 1) + 4 * h + q4) * 896 + 112 * (v + 4 * (i >> 2)) + 64 * ((i >> 1) & 1) + ((lane >> 4) & 1) * 32 + (lane & 3) * 8;
                f[i] = tr8(lds, a0, a0 + 8 * 896);
            }
            const u32 m24 = m < 24 ? ~0u : 0u, mh = (isd || h == 0) ? ~0u : 0u;
#pragma unroll
            for (int i = 0; i < 8; ++i) {
                const int jm = (v + 4 * (i >> 2)) * 2 + ((i >> 1) & 1), sk = i & 1;
                if (i & 2) f[i] &= m24;
                if (sk) { f[i][2] &= mh; f[i][3] &= mh; }
                stx(f[i], rs, l16, isd ? OFF_WD + (jm * 112 + 2 * jb + sk) * 1024 : OFF_WO + (jm * 32 + 2 * jb + sk) * 1024);
            }
        }
        VM0();
        publish(lane);
        STAMP_AT(15 + 16);
    };

    const u32 po = w < 4 ? OFF_WO + (j8 * 64 + 8 * w) * 1024 : OFF_WD + (j8 * 224 + 28 * (w - 4)) * 1024;
    const u32 l16o = l16;
    auto attend = [&](auto role, u32x4* P) {
        constexpr bool A = decltype(role)::value;
        u32 l16 = l16o;
        if (A) asm volatile("; waves 0-3" : "+v"(l16)); else asm volatile("; waves 4-7" : "+v"(l16));
    u32 kvo = (lay ? OFF_KV2 : OFF_KV) + w * 32 * 1024, pb = po, gb = OFF_WGU + (i16 * 196 + w * 21) * 1024, gl = lds0 + L_GU0 + w * 21 * 1024;
    auto kvld = [&](u32 off) { return ldx(rs, l16, kvo + off); };
    auto first_keys = [&](u32x4 (*kf)[4]) {
#pragma unroll
        for (int i = 0; i < 4; ++i) kf[0][i] = kvld(i * 1024);
        kf[1][0] = kvld(4096);
    };
    auto keys_complete = [&]() {
        for (int sp = 0; !wt.dead; ++sp) {
            u32 mx = 0;
            asm volatile("" ::: "memory");
#pragma unroll 1
            for (int i = 0; i < 32; i += 4) { u32x4 t[4];
#pragma unroll
                for (int e = 0; e < 4; ++e) t[e] = kvld((i + e) * 1024);
                mx = umax(mx, fold_all<2, 4>(t)); }
            if (all_written<2>(mx)) break;
            if (sp > SPIN_CAP) wt.dead = true;
        }
    };
    constexpr bool FIRST = lay ? !A : A;
    constexpr u32 lq = lay ? 0 : F_Q, lkn = lay ? L_KN : F_KN;
    if (FIRST) __builtin_amdgcn_s_setprio(1);
    u32x4 q[4][2], kf[2][4];
    u32 knv;
    if (lay == 0) {
        if (A) {
            const u32 pv = ((l16 >> 4) & 15) * 128 + ((l16 >> 8) & 1) * 8192;
            for (int sp = 0;; ++sp) {
                asm volatile("" ::: "memory");
                const u32x2 f = __builtin_bit_cast(u32x2, __builtin_amdgcn_raw_buffer_load_b64(rs, pv, OFF_F1 + w * 2048, 16));
                knv = f[1];
                if (__builtin_expect(__all(f[0] == token) || wt.dead, 1)) break;
                __builtin_amdgcn_s_sleep(1);
                if (sp > SPIN_CAP) wt.dead = true;
            }
            asm volatile("" ::: "memory");
            first_keys(kf);
            *(u32*)(smem + lkn + w * 128 + ((l16 >> 2) & 124)) = knv;
            arrive(lds, F_CNT + 28, l16);
        }
        wait_for(lds, F_CNT + 28, 4u);
        if (!A) first_keys(kf);
    } else {
        const u32 pv = ((l16 >> 4) & 7) * 4096 + ((l16 & 128) ? 3072 : 1024 + 32 * 16 + 12);
        for (int sp = 0;; ++sp) {
            asm volatile("" ::: "memory");
            knv = __builtin_amdgcn_raw_buffer_load_b32(rs, pv, kvo, 16);
            SB();
            first_keys(kf);
            asm volatile("" ::: "memory");
            if (__builtin_expect(all_written<2>(fold<2>(umax(knv, fold_all<2, 4>(kf[0])), kf[1][0])) || wt.dead, 1)) break;
            if (sp > SPIN_CAP) wt.dead = true;
        }
        *(u32*)(smem + lkn + (l16 < 128 ? w * 32 : 512) + (l16 >> 2)) = knv << 16;
        BAR();
    }
#pragma unroll
    for (int i = 0; i < 8; ++i) q[i >> 1][i & 1] = lds_ld(lds, lq + i * 1024 + l16);
    knv = *(const u32*)(smem + lkn + (l16 >> 2));
    if (lay == 0) knv = umax(knv, *(const u32*)(smem + lkn + 256 + (l16 >> 2)));
    bool checked = lay == 0;
    {
        FRESH_LANE();
        const float kn = __uint_as_float(wave_umax(knv));
        float sh[4]; bool safe = true;
#pragma unroll
        for (int t = 0; t < 4; ++t) { sh[t] = blo(q[t][1][2]) * kn; safe = safe && (h == 0 || sh[t] <= 48.f); }
        if (__builtin_expect(!__all(safe), 0)) {
            if (lay == 1) { keys_complete(); first_keys(kf); checked = true; }
            float* mx = (float*)(smem + 8192);
            float rm[4] = {-INFINITY, -INFINITY, -INFINITY, -INFINITY};
#pragma unroll
            for (int t = 0; t < 4; ++t) q[t][1][2] = h ? 0u : q[t][1][2];
#pragma unroll 1
            for (int kt = 0; kt < 8; ++kt) {
                const u32x4 k0 = kvld(kt * 4096), k1 = kvld(kt * 4096 + 1024);
#pragma unroll
                for (int t = 0; t < 4; ++t) {
                    f32x16 s = {}; s = mfma(k0, q[t][0], s); s = mfma(k1, q[t][1], s);
#pragma unroll
                    for (int r = 0; r < 16; ++r) rm[t] = fmaxf(rm[t], s[r]);
                }
            }
#pragma unroll
            for (int t = 0; t < 4; ++t) { rm[t] = xhalf_max(rm[t]); if (h == 0) mx[(w * 4 + t) * 32 + m] = rm[t]; }
            __syncthreads();
#pragma unroll
            for (int t = 0; t < 4; ++t) { float v = mx[t * 32 + m];
#pragma unroll
                for (int u = 1; u < 8; ++u) v = fmaxf(v, mx[(u * 4 + t) * 32 + m]);
                sh[t] = v; }
            __syncthreads();
        }
#pragma unroll
        for (int t = 0; t < 4; ++t) {
            const u32 hi = pk(-sh[t], 0.f) & 0xffffu; const u32 lo = pk(-sh[t] - blo(hi), 0.f);
            q[t][1][2] = h ? (hi | (lo << 16)) : q[t][1][2];
        }
    }
    STAMP_AT(1 + 16 * lay);
        f32x16 o[4];
        u32 seen[4] = {}; int spins = 0;
        for (;;) {
        asm volatile("" : "+s"(kvo), "+s"(pb), "+s"(gb), "+s"(gl));
#pragma unroll
        for (int t = 0; t < 4; ++t) o[t] = f32x16{};
        f32x16 sb[3];
        const f32x16 zero = {};
        sb[0] = mfma(kf[0][0], q[0][0], zero); sb[0] = mfma(kf[0][1], q[0][1], sb[0]);
#define EX1(x) do { float t_ = __builtin_amdgcn_exp2f(x); PIN(t_); x = t_; } while (0)
#define EX4(S, i) do { EX1(S[i]); EX1(S[i + 1]); EX1(S[i + 2]); EX1(S[i + 3]); SB(); } while (0)
        SB();
#pragma unroll
        for (int u = 0; u < 32; ++u) {
            const int kt = u >> 2, t = u & 3, un = u + 1, ktn = un >> 2, tn = un & 3, up = u - 1;
            f32x16& cur = sb[u % 3]; f32x16& nxt = sb[un % 3]; f32x16& prv = sb[(u + 2) % 3];
            if (un < 32) { nxt = mfma(kf[ktn & 1][0], q[tn][0], zero); PIN(nxt); }
            SB();
            if (lay == 0 && A && u == U2 - 5 && w == 0) {
#pragma unroll
                for (int i = 0; i < 4; ++i) seen[i] = __builtin_amdgcn_raw_buffer_load_b32(rs, l16 * 2, OFF_F2 + i * 2048, 16);
            }
            if (lay == 0 && A && u == U2 - 1 && w == 0) {
                while (!__all(seen[0] == token && seen[1] == token && seen[2] == token && seen[3] == token) && !wt.dead) {
                    asm volatile("" ::: "memory");
#pragma unroll
                    for (int i = 0; i < 4; ++i) seen[i] = __builtin_amdgcn_raw_buffer_load_b32(rs, l16 * 2, OFF_F2 + i * 2048, 16);
                    if (++spins > SPIN_CAP) wt.dead = true;
                }
                arrive(lds, F_CNT + 24, l16);
            }
            if (lay == 0 && u == (A ? U2 : 26)) wait_for(lds, F_CNT + 24, 1u);
            if (A) {
                constexpr int u0 = lay ? 0 : U2, n = lay ? 3 : 1;
#pragma unroll
                for (int i = n * (u - u0); i >= 0 && i < n * (u - u0) + n && i < 14; ++i) P[i] = ldx(rs, l16, pb + ((i >> 3) * 32 + (i & 7)) * 1024);
            }
            SB(); EX4(cur, 0);
            if (un < 32) { nxt = mfma(kf[ktn & 1][1], q[tn][1], nxt); PIN(nxt); }
            SB(); EX4(cur, 4);
            if (up >= 0) { o[up & 3] = smfmac_hi(kf[(up >> 2) & 1][3], __builtin_shufflevector(prv, prv, 8, 9, 10, 11, 12, 13, 14, 15), o[up & 3]); PIN(o[up & 3]); }
            SB();
            if (t == 3) { if (kt + 2 < 8) kf[kt & 1][0] = kvld((kt + 2) * 4096); }
            else if (kt + 1 < 8) kf[(kt + 1) & 1][t + 1] = kvld(((kt + 1) * 4 + t + 1) * 1024);
            SB(); EX4(cur, 8);
            o[t] = smfmac_hi(kf[kt & 1][2], __builtin_shufflevector(cur, cur, 0, 1, 2, 3, 4, 5, 6, 7), o[t]); PIN(o[t]);
            SB();
            if (A) {
                constexpr int u0 = lay ? 0 : U2, n = lay ? 3 : 2;
#pragma unroll
                for (int p = n * (u - u0); p >= 0 && p < n * (u - u0) + n && p < 21; ++p) dma(rs, l16, gb + p * 1024, gl + p * 1024);
            }
            if (u >= 26 && !A) {
#pragma unroll
                for (int i = 3 * (u - 26); i < 3 * (u - 26) + 3 && i < 16; ++i) P[i] = ldx(rs, l16, pb + ((i & 1) * 112 + 7 * (((i >> 1) + j8) & 3) + (i >> 3)) * 1024);
            }
            if (u == (lay ? FLIP : FLIP0) && FIRST) __builtin_amdgcn_s_setprio(0);
            if (lay == 0 && u == FLIP1 && !FIRST) __builtin_amdgcn_s_setprio(1);
            SB(); EX4(cur, 12);
        }
        o[3] = smfmac_hi(kf[1][3], __builtin_shufflevector(sb[31 % 3], sb[31 % 3], 8, 9, 10, 11, 12, 13, 14, 15), o[3]);
        if (lay == 0) break;
        float pz = o[0][0];
#pragma unroll
        for (int r = 1; r < 16; ++r) pz += o[0][r];
        if (__builtin_expect(checked || !__any(pz != pz), 1)) break;
        keys_complete(); first_keys(kf); checked = true;
        }
        STAMP_AT(2 + 16 * lay);
#pragma unroll
        for (int c = 0; c < 8; ++c) lds_st(lds, L_RED + (w * 8 + c) * 1024 + l16, pack8(o[c >> 1], c & 1));
    };
    if (w < 4) {
        u32x4 P[16], res[2];
        if (lay == 0) start_a();
        attend(std::true_type{}, P);
        SB();
        P[14] = ldx(rs, l16, po + 38 * 1024); P[15] = ldx(rs, l16, po + 39 * 1024);
        if (lay) { res[0] = ldx(rs, l16, o_x2 + xo0); res[1] = ldx(rs, l16, o_x2 + (xo1 & ~15u)); }
        else {
            FRESH_LANE();
            const uint16_t* r = in.x + (size_t)(32 * ntile + m) * D_ + 56 * j8 + 32 * mt + 4 * h;
            const u32x2 r0 = *(const u32x2*)r, r1 = *(const u32x2*)(r + 8), r2 = *(const u32x2*)(r + 16), r3 = *(const u32x2*)(r + (mt ? 16 : 24));
            res[0] = (u32x4){r0[0], r0[1], r1[0], r1[1]}; res[1] = (u32x4){r2[0], r2[1], r3[0], r3[1]};
        }
        SB();
        const Sel sel = make_sel(l16 >> 4);
        f32x16 acc0 = {}; PIN(acc0);
        BAR();
        STAMP_AT(3 + 16 * lay);
        {
            f32x16 acc = add_tiles<8>(sel, lds, L_RED + 2 * w * 1024 + l16, 8192, acc0);
            BAR();
            const auto sw = __builtin_amdgcn_permlane32_swap(__float_as_uint(acc[12]), __float_as_uint(acc[12]), false, false);
            const float il = __builtin_amdgcn_rcpf(__uint_as_float(sw[1]));
#pragma unroll
            for (int r = 0; r < 16; ++r) acc[r] *= il;
            const u32 oo = OFF_OF + ((g * 8 + j8) * 8 + 2 * w) * 1024;
            stx(mark<lay>(pack8(acc, 0)), rs, l16, oo); stx(mark<lay>(pack8(acc, 1)), rs, l16, oo + 1024);
        }
        STAMP_AT(4 + 16 * lay);
        VM0();
        STAMP_AT(5 + 16 * lay);
        u32x4 xr[2];
        {
            u32x4 ob[16];
            const u32 oo = OFF_OF + (g * 8 + 2 * w) * 8192;
            auto old = [&](int i) { const int e = (i >> 2) * 4 + (i & 1) * 2 + ((i >> 1) & 1); return ldx(rs, l16 + (e & 3) * 1024, oo + (e >> 2) * 4096); };
            gated<lay>(rs, ((l16 >> 4) & 7) * 2048 + 1024, oo, wt, [&]() {
#pragma unroll
                for (int i = 0; i < 10; ++i) ob[i] = old(i); });
#pragma unroll
            for (int i = 10; i < 16; ++i) ob[i] = old(i);
            if (mt) res[1] = (lay && (j8 & 1)) ? (u32x4){res[1][2], res[1][3], 0u, 0u} : (u32x4){res[1][0], res[1][1], 0u, 0u};
            f32x16 y0 = add_tile(sel, res[0], res[1], f32x16{}); PIN(y0);
            f32x16 acc[4] = {};
#pragma unroll
            for (int c = 0; c < 4; ++c) {
                settle<lay, 4>(ob + 4 * c, unwritten<lay>(fold_all<lay, 4>(ob + 4 * c)), wt, [&](int i) { return old(4 * c + i); });
#pragma unroll
                for (int i = 0; i < 4; ++i) { const int ks = 2 * c + (i >> 1), n = i & 1;
                    acc[n] = mfma(P[ks], ob[4 * c + i], acc[n]); acc[2 + n] = mfma(P[8 + ks], ob[4 * c + i], acc[2 + n]); }
            }
            STAMP_AT(6 + 16 * lay);
#pragma unroll
            for (int c = 0; c < 8; ++c) lds_st(lds, (w * 8 + c) * 1024 + l16, pack8(acc[c >> 1], c & 1));
            BAR();
            f32x16 y = add_tiles<4>(sel, lds, 2 * w * 1024 + l16, 8192, y0);
            BAR();
            xr[0] = pack8(y, 0); xr[1] = pack8(y, 1);
            stx(mark<lay>(xr[0]), rs, l16, o_x1 + xo0);
            if (mt) stx2((u32x2){mark1<lay>(xr[1][0]), mark1<lay>(xr[1][1])}, rs, l16, o_x1 + xo1); else stx(mark<lay>(xr[1]), rs, l16, o_x1 + xo1);
            float s1 = 0.f, s2 = 0.f;
#pragma unroll
            for (int r = 0; r < 16; ++r) { s1 += y[r]; s2 = fmaf(y[r], y[r], s2); }
            s1 = xhalf_sum(s1); s2 = xhalf_sum(s2);
            stx2((u32x2){mark1<lay>(__float_as_uint(s1)), mark1<lay>(__float_as_uint(s2))}, rs, l16 < 512 ? l16 : OOB, o_st1 + sto);
        }
        STAMP_AT(7 + 16 * lay);
        {
            __builtin_amdgcn_s_setprio(2);
            FRESH_LANE();
            const int nt = 4 * blk1 + w;
            const u32 xo = o_x1 + nt * 28 * 1024, so = o_st1 + nt * 4096 + 0u, sv = (4 * h) * 512 + m * 16;
            u32x4 x[28], sr[4];
            f32x16 a[4]; const f32x16 zero = {};
            u32x4 ar[6], br[RING];
            u32 lb = l16 + L_GU0, lc = l16 + L_GU0 + 56 * 1024, lcs = L_CS + h * 16; PIN(lb); PIN(lc); PIN(lcs);
            auto gu012 = [&](int tt, int ks) { return tt < 2 ? lds_ld(lds, lb + (tt * 28 + ks) * 1024) : lds_ld(lds, lc + ks * 1024); };
            auto gu36 = [&](int e) { return e < 28 ? lds_ld(lds, l16 + L_GU3 + e * 1024) : e < 56 ? lds_ld(lds, l16 + L_GU4 + (e - 28) * 1024) : lds_ld(lds, lb + (e - 56) * 1024); };
#pragma unroll
            for (int i = 0; i < 6; ++i) ar[i] = gu012(i % 3, i / 3);
            const int pj = lane & 7;
            const u32 pm = 0u - ((lane >> 3) & 1);
            u32 pv = 7168 * (pj >> 1) + ((6144 + 8 * (pj & 1)) & pm) + ((3072 * (pj & 1) + 1024) & ~pm); PIN(pv);
            SB();
            asm volatile("s_waitcnt vmcnt(1)" ::: "memory");
            STAMP_AT(8 + 16 * lay);
            gated<lay>(rs, pv, xo, wt, [&]() {
#pragma unroll
                for (int ks = 0; ks < 10; ++ks) x[ks] = ldx(rs, l16 + (ks & 3) * 1024, xo + (ks >> 2) * 4096); });
            settle<lay, 2>(x, unwritten<lay>(fold_all<lay, 2>(x)), wt, [&](int i) { return ldx(rs, l16, xo + i * 1024); });
            x[10] = ldx(rs, l16 + 2048, xo + 8192); x[11] = ldx(rs, l16 + 3072, xo + 8192);
            float nmean = 0.f;
            SB();
#pragma unroll
            for (int c = 0; c < 14; ++c) {
                u32 mx = 0; u64 bad = 0, bads = 0;
#pragma unroll
                for (int i = 0; i < 6; ++i) {
                    const int ks = 2 * c + i / 3, tt = i % 3, e = ks * 3 + tt;
                    a[tt] = mfma(ar[e % 6], x[ks], ks == 0 ? zero : a[tt]);
                    if (ks + 2 < 28) ar[e % 6] = gu012(tt, ks + 2); else if (i < RING) br[i] = gu36(i);
                    if (tt == 0) { if (ks + 12 < 28) x[ks + 12] = ldx(rs, l16 + (ks & 3) * 1024, xo + ((ks + 12) >> 2) * 4096); else if (ks < 20) sr[ks - 16] = ldx(rs, sv, so + (ks - 16) * 512); }
                    if (c < 13) {
                        if (i == 3) { mx = fold_all<lay, 2>(x + 2 * c + 2); PIN(mx); }
                        if (i == 4) bad = unwritten<lay>(mx);
                        if (c == 12 && i == 1) { mx = fold_all<lay, 4>(sr); PIN(mx); }
                        if (c == 12 && i == 2) bads = unwritten<lay>(mx);
                    } else {
                        if (i == 0) { nmean = (__uint_as_float(sr[0][0]) + __uint_as_float(sr[0][2])) + (__uint_as_float(sr[1][0]) + __uint_as_float(sr[1][2])); PIN(nmean); }
                        if (i == 1) { nmean += (__uint_as_float(sr[2][0]) + __uint_as_float(sr[2][2])) + (__uint_as_float(sr[3][0]) + __uint_as_float(sr[3][2])); PIN(nmean); }
                        if (i == 2) { nmean = xhalf_sum(nmean) * (-1.f / D_); PIN(nmean); }
                    }
                    SB();
                }
                if (c < 13) settle<lay, 2>(x + 2 * c + 2, bad, wt, [&](int i) { return ldx(rs, l16, xo + (2 * c + 2 + i) * 1024); });
                if (c == 12) settle<lay, 4>(sr, bads, wt, [&](int i) { return ldx(rs, sv, so + i * 512); });
                SB();
            }
            STAMP_AT(9 + 16 * lay);
            f32x4 cs[2][4]; float hv[2][8]; u32x4 hp[2];
            auto epi = [&](int z, int e, int k, f32x16& v) {
                if (k < 2) { cs[z][2 * k] = __builtin_bit_cast(f32x4, lds_ld(lds, lcs + e * 128 + (2 * k) * 32)); cs[z][2 * k + 1] = __builtin_bit_cast(f32x4, lds_ld(lds, lcs + e * 128 + (2 * k + 1) * 32)); }
                else if (k >= 3 && k < 11) { const int r = k - 3;
                    float gg = fmaf(nmean, cs[z][r >> 2][r & 3], v[r]), uu = v[r + 8];
                    asm volatile("" : "+v"(gg), "+v"(uu));
                    hv[z][r] = gg * fmaf(nmean, cs[z][2 + (r >> 2)][r & 3], uu); PIN(hv[z][r]); }
                else if (k == 11) { hp[z][0] = mark1<lay>(pk(hv[z][0], hv[z][1])); hp[z][1] = mark1<lay>(pk(hv[z][2], hv[z][3])); PIN(hp[z]); }
                else if (k == 12) { hp[z][2] = mark1<lay>(pk(hv[z][4], hv[z][5])); hp[z][3] = mark1<lay>(pk(hv[z][6], hv[z][7])); PIN(hp[z]); }
                else if (k == 13) stx(hp[z], rs, l16, o_hf + (nt * 112 + 7 * i16 + e) * 1024);
            };
#pragma unroll
            for (int tt = 3; tt < 7; ++tt) {
#pragma unroll
                for (int ks = 0; ks < 28; ++ks) {
                    const int e = (tt - 3) * 28 + ks;
                    a[tt & 3] = mfma(br[e % RING], x[ks], ks == 0 ? zero : a[tt & 3]);
                    if (e == 2) RAWBAR();
                    if (e == 56 - RING) RAWBAR();
                    if (e == 68) RAWBAR();
                    if (e + RING < 112) br[e % RING] = gu36(e + RING);
                    if (tt == 3) epi(ks / 14, ks / 14, ks % 14, a[ks / 14]);
                    else if (tt == 4) epi(ks / 14, 2 + ks / 14, ks % 14, a[2 + ks / 14]);
                    else if (ks >= 1 && ks < 15) epi(0, tt - 1, ks - 1, a[(tt - 1) & 3]);
                    else if (tt == 6 && ks >= 26) epi(0, 6, ks - 26, a[2]);
                    SB();
                }
            }
#pragma unroll
            for (int k = 2; k < 14; ++k) epi(0, 6, k, a[2]);
            __builtin_amdgcn_s_setprio(0);
        }
        STAMP_AT(10 + 16 * lay);
        const Sel sel2 = make_sel(l16 >> 4);
        float r2; f32x16 y0 = {}, xres;
        { FRESH_LANE();
          const u32 so = o_st1 + ntile * 4096, sv = (4 * h) * 512 + m * 16;
          u32x4 sr[4];
#pragma unroll
          for (int i = 0; i < 4; ++i) sr[i] = ldx(rs, sv, so + i * 512);
          settle<lay, 4>(sr, unwritten<lay>(fold_all<lay, 4>(sr)), wt, [&](int i) { return ldx(rs, sv, so + i * 512); });
          float mean, rstd; stats_from(sr, mean, rstd); r2 = rstd * rstd;
#pragma unroll
          for (int i = 0; i < 8; ++i) { xres[2 * i] = blo(xr[i >> 2][i & 3]); xres[2 * i + 1] = bhi(xr[i >> 2][i & 3]); } }
        u32 lpd = L_WD1 + 2 * w * 1024 + l16; PIN(lpd); PIN(r2); PIN(y0); PIN(xres);
        BAR();
        STAMP_AT(11 + 16 * lay);
        {
            f32x16 y = add_tiles<4>(sel2, lds, lpd, 8192, y0);
#pragma unroll
            for (int r = 0; r < 16; ++r) y[r] = fmaf(y[r], r2, xres[r]);
            if (lay == 1) {
                FRESH_LANE();
                const u32x4 p0 = pack8(y, 0), p1 = pack8(y, 1);
                uint16_t* r = out + (size_t)(32 * ntile + m) * D_ + 56 * j8 + 32 * mt + 4 * h;
                *(u32x2*)r = (u32x2){p0[0], p0[1]}; *(u32x2*)(r + 8) = (u32x2){p0[2], p0[3]}; *(u32x2*)(r + 16) = (u32x2){p1[0], p1[1]};
                if (mt == 0) *(u32x2*)(r + 24) = (u32x2){p1[2], p1[3]};
            } else {
            const u32x4 p0 = mark<2>(pack8(y, 0)), p1 = mark<2>(pack8(y, 1));
            stx(p0, rs, l16, o_x2 + xo0);
            if (mt) stx2((u32x2){p1[0], p1[1]}, rs, l16, o_x2 + xo1); else stx(p1, rs, l16, o_x2 + xo1);
            float s1 = 0.f, s2 = 0.f;
#pragma unroll
            for (int r = 0; r < 16; ++r) { s1 += y[r]; s2 = fmaf(y[r], y[r], s2); }
            s1 = xhalf_sum(s1); s2 = xhalf_sum(s2);
            stx2((u32x2){mark1<2>(__float_as_uint(s1)), mark1<2>(__float_as_uint(s2))}, rs, l16 < 512 ? l16 : OOB, o_st2 + sto);
            }
        }
        STAMP_AT(12 + 16 * lay);
        if (lay == 0) asm volatile("s_waitcnt vmcnt(1)" ::: "memory");
    } else {
        u32x4 P[16], P2[16];
        if (lay == 0) start_b();
        attend(std::false_type{}, P);
        if (lay == 0) __builtin_amdgcn_s_setprio(0);
        const int kq = w - 4;
        const u32 gu = OFF_WGU + (i16 * 196 + kq * 7) * 1024, wdo = OFF_WD + (j8 * 224 + 28 * kq) * 1024;
        auto wdf = [&](int i, int t0) { return ldx(rs, l16, wdo + ((i & 1) * 112 + 7 * (((i >> 1) + j8) & 3) + t0 + (i >> 3)) * 1024); };
        u32x4 P3[16];
        auto pre = [&](int i) { if (i < 16) P2[i] = wdf(i, 2); else P3[i - 16] = wdf(i - 16, 5); };
#pragma unroll
        for (int i = 0; i < NA; ++i) pre(i);
        BAR();
        STAMP_AT(3 + 16 * lay);
#pragma unroll
        for (int i = NA; i < NA + NB; ++i) pre(i);
        BAR();
#pragma unroll
        for (int i = 0; i < 7; ++i) dma(rs, l16, gu + (84 + i) * 1024, lds0 + L_GU3 + (kq * 7 + i) * 1024);
        if (lay == 0) { float cs = 0.f;
#pragma unroll
          for (int p = 0; p < 4; ++p) cs += __uint_as_float(__builtin_amdgcn_raw_buffer_load_b32(rs, l16 >> 2, OFF_CG + (p * 112 + 7 * i16) * 128 + kq * 224, 16));
          *(float*)(smem + (l16 < 56 * 16 ? L_CS + kq * 224 : L_KN + 512) + (l16 >> 2)) = cs; }
        VM0();
        BAR();
#pragma unroll
        for (int i = NA + NB; i < NA + NB + NC; ++i) pre(i);
        BAR();
#pragma unroll
        for (int i = 0; i < 7; ++i) dma(rs, l16, gu + (112 + i) * 1024, lds0 + L_GU4 + (kq * 7 + i) * 1024);
        __builtin_amdgcn_s_sleep(PS0);
#pragma unroll
        for (int i = NA + NB + NC; i < 32 - ND; ++i) { pre(i); SB(); __builtin_amdgcn_s_sleep(PS1); SB(); }
        asm volatile("s_waitcnt vmcnt(%0)" ::"i"(32 - ND - NA - NB - NC) : "memory");
        RAWBAR();
#pragma unroll
        for (int i = 0; i < 7; ++i) { dma(rs, l16, gu + (140 + i) * 1024, lds0 + L_GU5 + (kq * 7 + i) * 1024); if (DSL) __builtin_amdgcn_s_sleep(DSL); }
#pragma unroll
        for (int i = 0; i < 7; ++i) { dma(rs, l16, gu + (168 + i) * 1024, lds0 + L_GU6 + (kq * 7 + i) * 1024); if (DSL) __builtin_amdgcn_s_sleep(DSL); }
        asm volatile("s_waitcnt vmcnt(7)" ::: "memory");
        RAWBAR();
#pragma unroll
        for (int i = 0; i < 8; ++i) { dma(rs, l16, wdo + ((i & 1) * 112 + 7 * (((i >> 1) + j8) & 3) + 4) * 1024, lds0 + L_WD1 + (kq * 8 + i) * 1024); if (DSL) __builtin_amdgcn_s_sleep(DSL); }
#pragma unroll
        for (int i = 32 - ND; i < 32; ++i) pre(i);
        asm volatile("s_waitcnt vmcnt(%0)" ::"i"(8 + ND) : "memory");
        RAWBAR();
        STAMP_AT(8 + 16 * lay);
        {
            const u32 ho = o_hf + (2 * g * 112 + 28 * kq) * 1024;
            u32 hs[4][2], l16b = l16 + 4096; PIN(l16b);
#pragma unroll
            for (int i = 0; i < 8; ++i) { hs[i >> 1][i & 1] = ho + ((i & 1) * 112 + 7 * (((i >> 1) + j8) & 3)) * 1024; asm volatile("" : "+s"(hs[i >> 1][i & 1])); }
            auto hld = [&](int s, int n) { return SAMEH ? ldx(rs, l16, hs[s & 3][n]) : ldx(rs, (s < 16 ? l16 : l16b) + ((s >> 2) & 3) * 1024, hs[s & 3][n]); };
            u32x4 hb[28][2], al[2][2];
            auto window = [&](int s0, int s1, u64 bad) {
                if (__builtin_expect(bad != 0, 0)) {
                    for (int sp = 0; !wt.dead; ++sp) {
                        asm volatile("" ::: "memory");
#pragma unroll
                        for (int z = s0; z < s1 && z < 28; ++z) { hb[z][0] = hld(z, 0); hb[z][1] = hld(z, 1); }
                        __builtin_amdgcn_s_waitcnt(0x0F70);
                        if (all_written<lay>(fold_all<lay, 2>(hb[s0]))) break;
                        if (sp > SPIN_CAP) wt.dead = true;
                    }
                }
            };
#pragma unroll
            for (int s = 0; s < 5; ++s) { hb[s][0] = hld(s, 0); hb[s][1] = hld(s, 1); }
            SB();
#pragma unroll
            for (int i = 0; i < 16; ++i) lds_st(lds, L_WD2 + (kq * 16 + i) * 1024 + l16, P3[i]);
            SB();
            hb[5][0] = hld(5, 0); hb[5][1] = hld(5, 1);
            u32 lw1 = L_WD1 + kq * 8192 + l16, lw2 = L_WD2 + kq * 16384 + l16; PIN(lw1); PIN(lw2);
            f32x16 acc[4]; const f32x16 zero = {};
            window(0, 6, unwritten<lay>(fold_all<lay, 2>(hb[0])));
            STAMP_AT(9 + 16 * lay);
#pragma unroll
            for (int s = 0; s < 28; ++s) {
                const u32x4 a0 = s < 8 ? P[(2 * s) & 15] : s < 16 ? P2[(2 * s) & 15] : al[s & 1][0], a1 = s < 8 ? P[(2 * s + 1) & 15] : s < 16 ? P2[(2 * s + 1) & 15] : al[s & 1][1];
                const bool two = s >= 1 && s < 1 + DIST - 6;
                const int z = s < 1 ? 6 : two ? 5 + 2 * s : s + DIST;
                acc[0] = mfma(a0, hb[s][0], s == 0 ? zero : acc[0]);
                if (z < 28) hb[z][0] = hld(z, 0);
                if (two) hb[z][1] = hld(z, 1);
                SB();
                acc[2] = mfma(a1, hb[s][0], s == 0 ? zero : acc[2]);
                if (two) { hb[z + 1][0] = hld(z + 1, 0); hb[z + 1][1] = hld(z + 1, 1); } else if (z < 28) hb[z][1] = hld(z, 1);
                if (s + 1 >= 16 && s + 1 < 28) {
                    const u32 lo = s + 1 < 20 ? lw1 + 2 * (s + 1 - 16) * 1024 : lw2 + 2 * (s + 1 - 20) * 1024;
                    al[(s + 1) & 1][0] = lds_ld(lds, lo); al[(s + 1) & 1][1] = lds_ld(lds, lo + 1024);
                }
                SB();
                acc[1] = mfma(a0, hb[s][1], s == 0 ? zero : acc[1]);
                u64 bad = 0;
                if (s + 1 < 28) bad = unwritten<lay>(fold_all<lay, 2>(hb[s + 1]));
                SB();
                acc[3] = mfma(a1, hb[s][1], s == 0 ? zero : acc[3]);
                SB();
                if (s + 1 < 28) window(s + 1, two ? z + 2 : z + 1, bad);
                SB();
            }
            STAMP_AT(10 + 16 * lay);
#pragma unroll
            for (int c = 0; c < 8; ++c) lds_st(lds, L_WD1 + (kq * 8 + c) * 1024 + l16, pack8(acc[c >> 1], c & 1));
        }
        BAR();
        STAMP_AT(11 + 16 * lay);
        if (lay == 0) {
#pragma unroll
        for (int i = 0; i < 14; ++i) dma(rs, l16, OFF_WQ + (j8 * 56 + kq * 14 + i) * 1024, lds0 + L_WQ + (kq * 14 + i) * 1024);
        if (j8 < 4) {
#pragma unroll
            for (int i = 0; i < 7; ++i) dma(rs, l16, OFF_WKV + ((j8 >> 1) * 28 + kq * 7 + i) * 1024, lds0 + L_WKV + (kq * 7 + i) * 1024);
        }
        }
    }
    if (lay == 0) {
        FRESH_LANE();
        const int kq = w >> 1;
        const bool kvjob = j8 < 4 && nn == (j8 & 1), isv = j8 >> 1;
        const u32 xo = o_x2 + (ntile * 28 + 7 * kq) * 1024;
        const int sn = w < 4 ? ntile : 2 * g + (j8 & 1);
        const u32 so = o_st2 + sn * 4096, sv = (4 * h) * 512 + m * 16;
        const bool fin = w < 4 || (w == 4 && j8 < 4);
        const int kind = w < 4 ? 0 : (isv ? 2 : 1), tile = w < 4 ? 2 * j8 + mt : 16 + isv;
        u32x4 x[7], sr[4], wa[21]; f32x4 cs[4];
        asm volatile("s_barrier" ::: "memory");
#pragma unroll
        for (int i = 0; i < 7; ++i) x[i] = ldx(rs, l16, xo + i * 1024);
        asm volatile("s_waitcnt vmcnt(7)\n\ts_barrier" ::: "memory");
        if (fin) {
#pragma unroll
            for (int i = 0; i < 4; ++i) sr[i] = ldx(rs, sv, so + i * 512);
            load_cs(cs, kind, rs, tile, lane);
        }
#pragma unroll
        for (int i = 0; i < 7; ++i) { wa[i] = lds_ld(lds, L_WQ + (7 * kq + i) * 1024 + l16); wa[7 + i] = lds_ld(lds, L_WQ + (28 + 7 * kq + i) * 1024 + l16); }
        if (kvjob) {
#pragma unroll
            for (int i = 0; i < 7; ++i) wa[14 + i] = lds_ld(lds, L_WKV + (7 * kq + i) * 1024 + l16);
        }
        const Sel sel3 = make_sel(l16 >> 4);
        u32 lpe = L_PE + l16; PIN(lpe);
        SB();
        settle<2, 7>(x, unwritten<2>(fold_all<2, 7>(x)), wt, [&](int i) { return ldx(rs, l16, xo + i * 1024); });
        STAMP_AT(13 + 16 * lay);
        const f32x16 zero = {};
        f32x16 qa[2], ka;
        if (kvjob) {
            if (isv) {
#pragma unroll
                for (int i = 0; i < 7; ++i) ka = mfma(x[i], wa[14 + i], i ? ka : zero);
            } else {
#pragma unroll
                for (int i = 0; i < 7; ++i) ka = mfma(wa[14 + i], x[i], i ? ka : zero);
            }
        }
#pragma unroll
        for (int i = 0; i < 7; ++i) qa[0] = mfma(wa[i], x[i], i ? qa[0] : zero);
        if (kvjob) { lds_st(lds, lpe + (kq * 10 + 8) * 1024, pack8(ka, 0)); lds_st(lds, lpe + (kq * 10 + 9) * 1024, pack8(ka, 1)); }
#pragma unroll
        for (int i = 0; i < 7; ++i) qa[1] = mfma(wa[7 + i], x[i], i ? qa[1] : zero);
        float mean = 0.f, rstd = 0.f;
        if (fin) {
            settle<2, 4>(sr, unwritten<2>(fold_all<2, 4>(sr)), wt, [&](int i) { return ldx(rs, sv, so + i * 512); });
            stats_from(sr, mean, rstd);
        }
        PIN(mean); PIN(rstd);
#pragma unroll
        for (int c = 0; c < 4; ++c) lds_st(lds, lpe + (kq * 10 + (c >> 1) * 4 + nn * 2 + (c & 1)) * 1024, pack8(qa[c >> 1], c & 1));
        f32x16 y = {}; PIN(y);
        BAR();
        STAMP_AT(14 + 16 * lay);
        if (fin) {
        y = add_tiles<4>(sel3, lds, lpe + 2 * w * 1024, 10240, y);
        const u32 dst = w < 4 ? w * 2048 : (u32)OFF_KV2 + (sn * 4 + (isv ? 2 : 0)) * 1024;
        qkv_finish<false>(y, kind, mean, rstd, cs, rs, lds, dst, (float*)(smem + L_SC), lane);
        STAMP_AT(15 + 16 * lay);
        if (w == 4) VM0();
        }
        if (w == 4 && j8 >= 4) __builtin_amdgcn_s_sleep(KVSL);
        RAWBAR();
    }
    STAMP_DUMP();
}

__global__ void __launch_bounds__(512) k_main(const uint16_t* x, const uint16_t* wq, const uint16_t* wk, const uint16_t* wv, const uint16_t* wo,
                                              const uint16_t* wg, const uint16_t* wu, const uint16_t* wd, char* ws, uint16_t* out) {
    extern __shared__ __attribute__((aligned(16))) char smem[];
    Wait wt;
    const In in = {x, wq, wk, wv, wo, wg, wu, wd};
    const u32 token = *(const __attribute__((address_space(4))) u32*)(ws + OFF_EP) + 1u;
    layer<0>(ws, in, out, smem, wt, token);
    if (blockIdx.x == 0 && threadIdx.x == 0) *(volatile u32*)(ws + OFF_EP) = token;
    layer<1>(ws, in, out, smem, wt, token);
}

extern "C" void kernel_launch(void* const* d_in, const int* in_sizes, int n_in, void* d_out, int out_size, void* d_ws, size_t ws_size, hipStream_t stream) {
    (void)hipFuncSetAttribute((const void*)k_main, hipFuncAttributeMaxDynamicSharedMemorySize, L_TOTAL);
    k_main<<<256, 512, L_TOTAL, stream>>>((const uint16_t*)d_in[0], (const uint16_t*)d_in[1], (const uint16_t*)d_in[2], (const uint16_t*)d_in[3], (const uint16_t*)d_in[4],
                                          (const uint16_t*)d_in[6], (const uint16_t*)d_in[7], (const uint16_t*)d_in[8], (char*)d_ws, (uint16_t*)d_out);
}
```

```cpp
#include <hip/hip_runtime.h>
#include <hip/hip_bf16.h>
#include <stdint.h>
#include <type_traits>

#define FLIP 20
#define TOUCH 1
#define T45 8
#define T6 18
#define DIST 6
#define SAMEH 0
#define P3AT 4
#define PS0 19
#define PS1 2
#define RING 4
#define DSL 0
#define NOHST 0
#define CODEPF 1
#define MAP 2
#define KVSL 34
#define ARMAT 0
#define RSL 30
#define FLIP0 26
#define ARMK 0
#define TWOST 0
#define NA 8
#define NB 8
#define NC 0
#define ND 0
#define HF0 7
#define RWAIT 2
#define FLIP1 0
#define CODEKB 90
#define U2 18
#define HFA 0
#define UH 0
#define HS 1
#define POLLQ 1
#define JOBAT 1
#define STGATE 0
typedef unsigned u32;
typedef u32 u32x2 __attribute__((ext_vector_type(2)));
typedef u32 u32x4 __attribute__((ext_vector_type(4)));
typedef float f32x2 __attribute__((ext_vector_type(2)));
typedef float f32x4 __attribute__((ext_vector_type(4)));
typedef float f32x8 __attribute__((ext_vector_type(8)));
typedef float f32x16 __attribute__((ext_vector_type(16)));
typedef __bf16 bf16x2 __attribute__((ext_vector_type(2)));
typedef __bf16 bf16x8 __attribute__((ext_vector_type(8)));
typedef __bf16 bf16x16 __attribute__((ext_vector_type(16)));
#define DI __device__ __forceinline__

constexpr int S_ = 2048, D_ = 448, F_ = 1792, DH = 28;
constexpr size_t KB = 1024;
constexpr size_t OFF_WQ = 0;
constexpr size_t OFF_WKV = OFF_WQ + 448 * KB;
constexpr size_t OFF_WO = OFF_WKV + 56 * KB;
constexpr size_t OFF_WGU = OFF_WO + 512 * KB;
constexpr size_t OFF_WD = OFF_WGU + 3136 * KB;
constexpr size_t OFF_CS = OFF_WD + 1792 * KB;
constexpr size_t OFF_CG = OFF_CS + 4 * KB;
constexpr size_t OFF_KV = OFF_CG + 56 * KB;
constexpr size_t OFF_F1 = OFF_KV + 256 * KB;
constexpr size_t OFF_F2 = OFF_F1 + 16 * KB;
constexpr size_t OFF_EP = OFF_F2 + 8 * KB;
constexpr size_t OFF_HO = OFF_EP + 1 * KB;
constexpr size_t OFF_OF = OFF_HO;
constexpr size_t OFF_X1 = OFF_OF + 2048 * KB;
constexpr size_t OFF_S1 = OFF_X1 + 1792 * KB;
constexpr size_t OFF_KV2 = OFF_S1 + 256 * KB;
constexpr size_t OFF_X2 = OFF_KV2 + 256 * KB;
constexpr size_t OFF_S2 = OFF_X2 + 1792 * KB;
constexpr size_t OFF_HF = OFF_S2 + 256 * KB;
constexpr size_t WS_END = OFF_HF + 7168 * KB;
static_assert((OFF_X2 - OFF_HO) / KB == 256 * 17, "17 KiB per workgroup");

#define STAMP_AT(i)
#define STAMP_INIT()
#define STAMP_DUMP()
DI f32x16 mfma(u32x4 a, u32x4 b, f32x16 c) {
    return __builtin_amdgcn_mfma_f32_32x32x16_bf16(__builtin_bit_cast(bf16x8, a), __builtin_bit_cast(bf16x8, b), c, 0, 0, 0);
}
DI f32x16 smfmac_hi(u32x4 a, f32x8 b, f32x16 c) {
    return __builtin_amdgcn_smfmac_f32_32x32x32_bf16(__builtin_bit_cast(bf16x8, a), __builtin_bit_cast(bf16x16, b), c, 0xDDDD, 0, 0);
}
DI u32 pk(float lo, float hi) { f32x2 v = {lo, hi}; return __builtin_bit_cast(u32, __builtin_convertvector(v, bf16x2)); }
DI float dot2(u32 a, u32 b, float c) { return __builtin_amdgcn_fdot2_f32_bf16(__builtin_bit_cast(bf16x2, a), __builtin_bit_cast(bf16x2, b), c, false); }
DI float blo(u32 x) { return __uint_as_float(x << 16); }
DI float bhi(u32 x) { return __uint_as_float(x & 0xffff0000u); }
DI float xhalf_sum(float v) { auto r = __builtin_amdgcn_permlane32_swap(__float_as_uint(v), __float_as_uint(v), false, false); return __uint_as_float(r[0]) + __uint_as_float(r[1]); }
DI float xhalf_max(float v) { auto r = __builtin_amdgcn_permlane32_swap(__float_as_uint(v), __float_as_uint(v), false, false); return fmaxf(__uint_as_float(r[0]), __uint_as_float(r[1])); }
DI u32x4 pack8(const f32x16& x, int s) { return (u32x4){pk(x[8 * s], x[8 * s + 1]), pk(x[8 * s + 2], x[8 * s + 3]), pk(x[8 * s + 4], x[8 * s + 5]), pk(x[8 * s + 6], x[8 * s + 7])}; }
DI int prow(int h, int j) { return 8 * (j >> 2) + 4 * h + (j & 3); }
DI int feat(int ks, int h, int j) {
    const int p = ks / 7, s = ks % 7;
    if (s < 6) return 112 * p + (s >= 3 ? 56 + 16 * (s - 3) : 16 * s) + prow(h, j);
    return 112 * p + 56 * (j >> 2) + 48 + 4 * h + (j & 3);
}

DI void ln_stats(float s1, float s2, float& mean, float& rstd) {
    s1 = xhalf_sum(s1); s2 = xhalf_sum(s2);
    mean = s1 * (1.f / D_);
    const float var = fmaxf(s2 * (1.f / D_) - mean * mean, 0.f);
    rstd = __builtin_amdgcn_rsqf(var + 1e-5f);
}

typedef __amdgpu_buffer_rsrc_t rsrc_t;
typedef __attribute__((address_space(3))) char* lds_t;
constexpr u32 OOB = 0x80000000u;
constexpr u32 ARMED = 0xFFFFFFFFu;
DI u32x4 ldx(rsrc_t r, u32 v, u32 s) { return __builtin_amdgcn_raw_buffer_load_b128(r, v, s, 16); }
#define HOLD(x) asm volatile("s_nop 1" ::"v"(x))
DI void st(u32x4 x, rsrc_t r, u32 v, u32 s) { __builtin_amdgcn_raw_buffer_store_b128(x, r, v, s, 0); HOLD(x); }
DI void stx(u32x4 x, rsrc_t r, u32 v, u32 s) { __builtin_amdgcn_raw_buffer_store_b128(x, r, v, s, 16); HOLD(x); }
DI void stx2(u32x2 x, rsrc_t r, u32 v, u32 s) { __builtin_amdgcn_raw_buffer_store_b64(x, r, v, s, 16); }
DI void dma(rsrc_t r, u32 v, u32 s, u32 l) { asm volatile("s_mov_b32 m0, %0\n\ts_nop 0\n\tbuffer_load_dwordx4 %1, %2, %3 offen sc1 lds" ::"s"(l), "v"(v), "s"(r), "s"(s) : "m0", "memory"); }
DI u32x4 lds_ld(lds_t p, u32 off) { return *(__attribute__((address_space(3))) u32x4*)(p + off); }
DI void lds_st(lds_t p, u32 off, u32x4 v) { *(__attribute__((address_space(3))) u32x4*)(p + off) = v; }
DI u32 umax(u32 a, u32 b) { return a > b ? a : b; }
DI u32 umin(u32 a, u32 b) { return a < b ? a : b; }
template <int MODE> DI u32 mark1(u32 a) { return MODE == 0 ? (a & ~1u) : MODE == 1 ? (a | 1u) : umin(a, ARMED - 1); }
template <int MODE> DI u32x4 mark(u32x4 a) { return (u32x4){mark1<MODE>(a[0]), mark1<MODE>(a[1]), mark1<MODE>(a[2]), mark1<MODE>(a[3])}; }
template <int MODE> DI u32 fold(u32 m, u32x4 a) {
    return MODE == 0 ? ((m | a[0] | a[1]) | a[2] | a[3]) : MODE == 1 ? ((m & a[0] & a[1]) & a[2] & a[3]) : umax(umax(umax(m, a[0]), a[1]), umax(a[2], a[3]));
}
template <int MODE, int N> DI u32 fold_all(const u32x4* x) {
    u32 m = MODE == 1 ? ~0u : 0u;
#pragma unroll
    for (int i = 0; i < N; ++i) m = fold<MODE>(m, x[i]);
    return m;
}
template <int MODE> DI bool all_written(u32 m) { return __all(MODE == 0 ? (m & 1u) == 0u : MODE == 1 ? (m & 1u) != 0u : m != ARMED); }
typedef unsigned long long u64;
template <int MODE> DI u64 unwritten(u32 m) { u64 b = __ballot(MODE == 0 ? (m & 1u) != 0u : MODE == 1 ? (m & 1u) == 0u : m == ARMED); asm volatile("" : "+s"(b)); return b; }
#define SB() __builtin_amdgcn_sched_barrier(0)
#define RAWBAR() asm volatile("s_barrier" ::: "memory")
#define PIN(x) asm volatile("" : "+v"(x))
#define BAR() asm volatile("s_waitcnt lgkmcnt(0)\n\ts_barrier" ::: "memory")
#define VM0() asm volatile("s_waitcnt vmcnt(0)" ::: "memory")
constexpr int SPIN_CAP = 1 << 15;
#define COUNT_RETRY()
struct Wait { bool dead = false; int retries = 0; };
template <int MODE, int N, class F> DI void settle(u32x4* x, u64 bad, Wait& wt, F&& reload) {
    if (__builtin_expect(bad != 0, 0)) {
        for (int sp = 0; !wt.dead; ++sp) {
            __builtin_amdgcn_s_sleep(1);
            asm volatile("" ::: "memory");
#pragma unroll
            for (int i = 0; i < N; ++i) x[i] = reload(i);
            if (all_written<MODE>(fold_all<MODE, N>(x))) break;
            if (sp > SPIN_CAP) wt.dead = true;
        }
    }
}
template <int MODE, class F> DI void gated(rsrc_t rs, u32 pv, u32 ps, Wait& wt, F&& batch) {
    for (int sp = 0;; ++sp) {
        asm volatile("" ::: "memory");
        const u32 p = __builtin_amdgcn_raw_buffer_load_b32(rs, pv, ps, 16);
        batch();
        asm volatile("" ::: "memory");
        if (__builtin_expect(all_written<MODE>(p) || wt.dead, 1)) break;
        if (sp > SPIN_CAP) wt.dead = true;
    }
}
DI u32 wave_umax(u32 v) {
    v = umax(v, __builtin_amdgcn_update_dpp(0u, v, 0x111, 0xf, 0xf, false));
    v = umax(v, __builtin_amdgcn_update_dpp(0u, v, 0x112, 0xf, 0xf, false));
    v = umax(v, __builtin_amdgcn_update_dpp(0u, v, 0x114, 0xf, 0xf, false));
    v = umax(v, __builtin_amdgcn_update_dpp(0u, v, 0x118, 0xf, 0xf, false));
    v = umax(v, __builtin_amdgcn_update_dpp(0u, v, 0x142, 0xa, 0xf, false));
    v = umax(v, __builtin_amdgcn_update_dpp(0u, v, 0x143, 0xc, 0xf, false));
    return __builtin_amdgcn_readlane(v, 63);
}

DI void load_cs(f32x4* c, int kind, rsrc_t rs, int tile, int lane) {
    if (kind != 2) {
#pragma unroll
        for (int i = 0; i < 4; ++i) c[i] = __builtin_bit_cast(f32x4, ldx(rs, (lane >> 5) * 16, OFF_CS + tile * 128 + i * 32));
    } else c[0][0] = __uint_as_float(__builtin_amdgcn_raw_buffer_load_b32(rs, (lane & 31) * 4, OFF_CS + tile * 128, 16));
}
template <bool NORMED> DI u32 qkv_finish(f32x16 acc, int kind, float mean, float rstd, const f32x4* cs, rsrc_t rs, lds_t lds, u32 dst, float* sc, int lane) {
    const int m = lane & 31, h = lane >> 5;
    u32 kb = 0;
    if (kind != 2) {
        const float s = kind == 0 ? rstd * (0.18898223650461362f * 1.4426950408889634f) : rstd;
#pragma unroll
        for (int r = 0; r < 16; ++r) acc[r] = NORMED ? acc[r] * s : (acc[r] - mean * cs[r >> 2][r & 3]) * s;
        u32x4 k0 = pack8(acc, 0), k1 = pack8(acc, 1);
        float nn = 0.f, n2 = 0.f;
#pragma unroll
        for (int i = 0; i < 4; ++i) { nn = dot2(k0[i], k0[i], nn); n2 = dot2(k1[i], k1[i], n2); }
        nn = xhalf_sum(nn + n2);
        if (kind == 0) {
            k1[2] = h ? pk(__builtin_amdgcn_sqrtf(nn) * 1.01f, 0.f) : k1[2];
            lds_st(lds, dst + lane * 16, k0); lds_st(lds, dst + lane * 16 + 1024, k1);
        } else {
            kb = pk(__builtin_amdgcn_sqrtf(__uint_as_float(wave_umax(__float_as_uint(nn)))) * 1.01f, 0.f);
            k1[2] = h ? 0x3f803f80u : k1[2];
            k1[3] = h ? kb : k1[3];
            const int slot = (m & 19) | ((m & 4) << 1) | ((m & 8) >> 1);
            stx(mark<2>(k0), rs, (slot + 32 * h) * 16, dst); stx(mark<2>(k1), rs, (slot + 32 * h) * 16, dst + 1024);
        }
    } else {
        sc[lane] = h ? rstd : mean;
        __builtin_amdgcn_fence(__ATOMIC_RELEASE, "wavefront"); __builtin_amdgcn_wave_barrier(); __builtin_amdgcn_fence(__ATOMIC_ACQUIRE, "wavefront");
#pragma unroll
        for (int i = 0; i < 4; ++i) {
            const f32x4 mu = *(const f32x4*)(sc + 8 * i + 4 * h), rr = *(const f32x4*)(sc + 32 + 8 * i + 4 * h);
#pragma unroll
            for (int r = 0; r < 4; ++r) acc[4 * i + r] = m < 28 ? (NORMED ? acc[4 * i + r] : acc[4 * i + r] - mu[r] * cs[0][0]) * rr[r] : (m == 28 ? 1.f : 0.f);
        }
        stx(mark<2>(pack8(acc, 0)), rs, lane * 16, dst); stx(mark<2>(pack8(acc, 1)), rs, lane * 16, dst + 1024);
    }
    return kb;
}

DI void stats_from(const u32x4* s, float& mean, float& rstd) {
    float s1 = 0.f, s2 = 0.f;
#pragma unroll
    for (int i = 0; i < 4; ++i) { s1 += __uint_as_float(s[i][0]) + __uint_as_float(s[i][2]); s2 += __uint_as_float(s[i][1]) + __uint_as_float(s[i][3]); }
    ln_stats(s1, s2, mean, rstd);
}

struct Sel { u32x4 a; u32 idx; };
DI Sel make_sel(int lane) {
    asm volatile("" : "+v"(lane));
    const int m = lane & 31, grp = ((m >> 1) & 2) | ((m >> 3) & 1);
    const u32 one = (m >> 4) == (lane >> 5) ? 0x3f80u : 0u;
    Sel s; s.idx = (m & 3) * 0x5555u;
#pragma unroll
    for (int i = 0; i < 4; ++i) s.a[i] = grp == i ? one : 0u;
    PIN(s.a); PIN(s.idx);
    return s;
}
DI f32x16 add_tile(const Sel& s, u32x4 lo, u32x4 hi, f32x16 c) {
    return __builtin_amdgcn_smfmac_f32_32x32x32_bf16(__builtin_bit_cast(bf16x8, s.a), __builtin_bit_cast(bf16x16, __builtin_shufflevector(lo, hi, 0, 1, 2, 3, 4, 5, 6, 7)), c, s.idx, 0, 0);
}
template <int N> DI f32x16 add_tiles(const Sel& s, lds_t lds, u32 off, u32 stride, f32x16 acc) {
    u32x4 t[N][2];
#pragma unroll
    for (int u = 0; u < N; ++u) { t[u][0] = lds_ld(lds, off + u * stride); t[u][1] = lds_ld(lds, off + u * stride + 1024); }
    SB();
#pragma unroll
    for (int u = 0; u < N; ++u) acc = add_tile(s, t[u][0], t[u][1], acc);
    return acc;
}

typedef short s16x4 __attribute__((ext_vector_type(4)));
DI u32x4 tr8(lds_t lds, u32 pa, u32 pb) {
    const u32x2 lo = __builtin_bit_cast(u32x2, __builtin_amdgcn_ds_read_tr16_b64_v4i16((__attribute__((address_space(3))) s16x4*)(lds + pa)));
    const u32x2 hi = __builtin_bit_cast(u32x2, __builtin_amdgcn_ds_read_tr16_b64_v4i16((__attribute__((address_space(3))) s16x4*)(lds + pb)));
    return (u32x4){lo[0], lo[1], hi[0], hi[1]};
}
constexpr u32 F_X = 0, F_WQ = 57344, F_WK = F_WQ + 50432, F_JOB = F_WK + 25600, F_Q = 40 * 1024, F_KN = 160 * 1024 - 640, F_TOTAL = 160 * 1024;
constexpr u32 F_UP = 14336 + 64;
constexpr u32 F_CNT = F_TOTAL - 128;
static_assert(F_JOB + F_UP + 14336 + 1024 <= F_KN, "LDS");
DI rsrc_t srd(const void* p, int bytes) { return __builtin_amdgcn_make_buffer_rsrc((void*)p, 0, bytes, 0x00020000); }
DI int fblk(int s, int half) { return s < 6 ? (s >= 3 ? 56 + 16 * (s - 3) : 16 * s) + 8 * half : 56 * half + 48; }
typedef __attribute__((address_space(3))) u32* ldsw_t;
DI u32 arrive(lds_t lds, u32 off, int lane) {
    asm volatile("s_waitcnt lgkmcnt(0)" ::: "memory");
    const u32 old = __hip_atomic_fetch_add((ldsw_t)(lds + off + (lane ? 64 : 0)), 1u, __ATOMIC_RELAXED, __HIP_MEMORY_SCOPE_WORKGROUP);
    asm volatile("" ::: "memory");
    return __builtin_amdgcn_readfirstlane(old);
}
DI void wait_for(lds_t lds, u32 off, u32 n) {
    while (__builtin_amdgcn_readfirstlane(__hip_atomic_load((ldsw_t)(lds + off), __ATOMIC_RELAXED, __HIP_MEMORY_SCOPE_WORKGROUP)) < n) __builtin_amdgcn_s_sleep(0);
    asm volatile("" ::: "memory");
}
struct In { const uint16_t *x, *wq, *wk, *wv, *wo, *wg, *wu, *wd; };

constexpr u32 L_RED = 0, L_GU0 = 64 * 1024, L_GU3 = 32 * 1024, L_GU4 = 0, L_GU5 = 64 * 1024, L_GU6 = 92 * 1024, L_WD1 = 120 * 1024, L_WD2 = 0,
              L_CS = 152 * 1024, L_SC = 154 * 1024, L_KN = 155 * 1024, L_WQ = 0, L_WKV = 56 * 1024, L_PE = 84 * 1024, L_TOTAL = 160 * 1024;

DI void job_fetch(const In& in, u32 lds0, int b, int v, int lane) {
    if (b < 112) {
        const rsrc_t rg = srd(v < 2 ? in.wg : in.wu, D_ * F_ * 2);
        const u32 gv = (lane >> 3) * 3584 + (lane & 7) * 16;
#pragma unroll
        for (int i = 0; i < 7; ++i) { const int p = (v & 1) + 2 * i; dma(rg, gv, (112 * (b & 3) + 8 * p) * 3584 + 128 * (b >> 2), lds0 + F_JOB + (v >> 1) * F_UP + p * 1024); }
    } else {
        const rsrc_t rd = srd(b < 168 ? in.wd : in.wo, b < 168 ? F_ * D_ * 2 : b < 184 ? D_ * D_ * 2 : 0);
        const u32 s0 = b < 168 ? (b - 112) * 28672 : (b - 168) * 25088;
#pragma unroll
        for (int i = 0; i < 7; ++i) dma(rd, lane * 16, s0 + (v + 4 * i) * 1024, lds0 + F_JOB + (v + 4 * i) * 1024);
    }
}
DI u32 first_fetch(const In& in, char* ws, char* smem) {
    const u32 lds0 = (u32)(uintptr_t)(lds_t)smem;
    u32x4 code;
    { const u32 piece = __builtin_amdgcn_readfirstlane((((blockIdx.x >> 3) * 8 + (threadIdx.x >> 6)) % CODEKB) * 1024), ll = (threadIdx.x & 63) * 16;
      asm volatile("s_getpc_b64 s[20:21]\n\ts_and_b32 s20, s20, -16\n\ts_add_u32 s20, s20, %1\n\ts_addc_u32 s21, s21, 0\n\tglobal_load_dwordx4 %0, %2, s[20:21]"
                   : "=&v"(code) : "s"(piece), "v"(ll) : "s20", "s21", "scc", "memory"); }
    const int b = blockIdx.x, w = __builtin_amdgcn_readfirstlane(threadIdx.x >> 6), lane = threadIdx.x & 63, g = (b & 7) * 4 + (b >> 6), j8 = (b >> 3) & 7;
    const rsrc_t rx = srd(in.x, S_ * D_ * 2), rq = srd(in.wq, D_ * D_ * 2), rk = srd(j8 & 2 ? in.wv : in.wk, j8 < 4 ? D_ * DH * 2 : 0);
    const u32 l16 = lane * 16, xv = (lane >> 3) * 128 + (((lane & 7) ^ (lane >> 4)) * 16), qv = (lane / 7) * 896 + (lane % 7) * 16;
    auto xp = [&](int e) { int p = e + 7 * j8; p = p >= 56 ? p - 56 : p; dma(rx, xv ^ ((p & 1) * 64), g * 57344 + p * 1024, lds0 + F_X + p * 1024); };
    auto qp = [&](int e) { int p = e + 12 * (g & 3); p = p >= 50 ? p - 50 : p; dma(rq, qv, p * 8064 + 112 * j8, lds0 + F_WQ + p * 1008); };
    auto kp = [&](int e) { int p = e + 3 * (2 * (g & 3) + (j8 & 1)); p = p >= 25 ? p - 25 : p; dma(rk, l16, p * 1024, lds0 + F_WK + p * 1024); };
    u32 token = *(const __attribute__((address_space(4))) u32*)(ws + OFF_EP);
    xp(w); kp(w); qp(w); qp(8 + (w & 1));
    if (threadIdx.x < 32) *(u32*)(smem + F_CNT + threadIdx.x * 4) = 0u;
    BAR();
    asm volatile("" : "+s"(token));
    if (w < 4) {
#pragma unroll
        for (int i = 2; i < 14; ++i) xp(4 * i + w);
#pragma unroll
        for (int i = 2; i < 6; ++i) kp(4 * i + w);
        kp(24);
        asm volatile("s_waitcnt vmcnt(0)" : "+v"(code)::"memory");
        arrive((lds_t)smem, F_CNT, lane);
    } else {
        const int v = w - 4;
        if (JOBAT == 3) { job_fetch(in, lds0, b, v, lane); asm volatile("s_waitcnt vmcnt(7)" : "+v"(code)::"memory"); }
        else asm volatile("s_waitcnt vmcnt(0)" : "+v"(code)::"memory");
        arrive((lds_t)smem, F_CNT, lane);
        if (JOBAT == 0) job_fetch(in, lds0, b, v, lane);
#pragma unroll
        for (int i = 0; i < 10; ++i) qp(6 + 4 * i + w);
        if (JOBAT == 1) job_fetch(in, lds0, b, v, lane);
    }
    return token + 1u;
}

template <int lay> DI void layer(char* ws, const In& in, uint16_t* out, char* smem, Wait& wt, u32 token) {
    const lds_t lds = (lds_t)smem;
    const u32 lds0 = (u32)(uintptr_t)lds;
    int b = blockIdx.x, w = __builtin_amdgcn_readfirstlane(threadIdx.x >> 6);
    asm volatile("" : "+s"(b), "+s"(w));
    const int g = (b & 7) * 4 + (b >> 6), j8 = (b >> 3) & 7;
    const int blk1 = MAP == 2 ? (b & 1) * 8 + ((b >> 3) & 7) : MAP == 1 ? ((b >> 1) & 3) * 4 + ((b >> 3) & 3) : g >> 1;
    const int i16 = MAP == 2 ? ((b >> 1) & 3) * 4 + (b >> 6) : MAP == 1 ? (b & 1) * 8 + (b >> 5) : (b >> 3) & 15;
    u32 l16 = __builtin_amdgcn_mbcnt_hi(~0u, __builtin_amdgcn_mbcnt_lo(~0u, 0u)) * 16; PIN(l16);
#define FRESH_LANE() u32 l16_ = l16; PIN(l16_); const int lane = l16_ >> 4, m = lane & 31, h = lane >> 5; (void)m; (void)h
    const rsrc_t rs = __builtin_amdgcn_make_buffer_rsrc(ws, 0, (int)WS_END, 0x00020000);
    constexpr u32 o_x1 = OFF_X1, o_x2 = OFF_X2, o_st1 = OFF_S1, o_st2 = OFF_S2, o_hf = OFF_HF;
    const int mt = (w >> 1) & 1, nn = w & 1, ntile = 2 * g + nn;
    const int ks0 = 7 * (j8 >> 1) + 3 * (j8 & 1), ksh = 7 * (j8 >> 1) + 6;
    const u32 xo0 = (ntile * 28 + ks0 + 2 * mt) * 1024, xo1 = mt ? (ntile * 28 + ksh) * 1024 + 8 * (j8 & 1) : xo0 + 1024;
    const u32 sto = ntile * 4096 + j8 * 512 + mt * 8;
    if (lay) STAMP_AT(16);

    const bool isg = b < 112, isd = !isg && b < 168;
    const int jb = isg ? b : isd ? b - 112 : b - 168;
    auto publish = [&](int lane) {
        const u32 old = arrive(lds, F_CNT + 16, lane);
        __builtin_amdgcn_raw_buffer_store_b32(token, rs, (lane || old != 7u) ? OOB : 0u, OFF_F2 + b * 32, 16);
    };
    constexpr u32 QK = 6272, QQ = 12544;
    auto finish = [&](const Sel& sel, int lane, u32 tile, u32 csum, u32 stride, int n, int kind, u32 dst) {
        const int m = lane & 31, h = lane >> 5;
        float s1 = 0.f, s2 = 0.f;
#pragma unroll
        for (int u = 0; u < 4; ++u) { const f32x2 t = *(const f32x2*)(smem + F_WK + u * QK + 2048 + n * 256 + m * 8); s1 += t[0]; s2 += t[1]; }
        const float c0 = *(const float*)(smem + csum + 2 * h * stride + m * 4), c1 = *(const float*)(smem + csum + (2 * h + 1) * stride + m * 4);
        f32x16 y = add_tiles<4>(sel, lds, tile + lane * 16, stride, f32x16{});
        const float nm = s1 * (-1.f / D_), rstd = __builtin_amdgcn_rsqf(fmaxf(s2 * (1.f / D_) - nm * nm, 0.f) + 1e-5f);
        const u32 mh = pk(nm, 0.f), mm = mh | (pk(nm - blo(mh), 0.f) << 16), h0 = pk(c0, c0), h1 = pk(c1, c1);
        const u32x4 fm = {mm, mm, mm, mm}, fc = {h0, pk(c0 - blo(h0), c0 - blo(h0)), h1, pk(c1 - blo(h1), c1 - blo(h1))};
        y = kind == 2 ? mfma(fm, fc, y) : mfma(fc, fm, y);
        return qkv_finish<true>(y, kind, 0.f, rstd, nullptr, rs, lds, dst, (float*)(smem + F_WK + 3072), lane);
    };
    auto x_rows = [&](u32x4 (*xf)[7], int lane) {
        const int m = lane & 31, h = lane >> 5;
        u32 xa[7];
#pragma unroll
        for (int i = 0; i < 7; ++i) { const int ch = 14 * (w & 3) + 2 * i + h, ln = 7 * m + (ch >> 3); xa[i] = F_X + ln * 128 + 16 * ((ch ^ (ln >> 1)) & 7); PIN(xa[i]); }
        wait_for(lds, F_CNT, 8u);
#pragma unroll
        for (int i = 0; i < 7; ++i) { xf[0][i] = lds_ld(lds, xa[i]); xf[1][i] = lds_ld(lds, xa[i] + 28672); }
    };
    auto start_a = [&]() {
        FRESH_LANE();
        __builtin_amdgcn_s_setprio(1);
        const int kq = w;
        const bool kvwg = j8 < 4, isv = j8 >> 1;
        u32 rk = F_WK + (112 * kq + 8 * h + ((lane & 15) >> 2)) * 56 + ((lane >> 4) & 1) * 32 + (lane & 3) * 8; PIN(rk);
        const Sel sel = make_sel(lane);
        u32x4 xf[2][7], ak[7];
        x_rows(xf, lane);
        STAMP_AT(14 + 16);
        if (kvwg) {
#pragma unroll
            for (int i = 0; i < 7; ++i) ak[i] = tr8(lds, rk + i * 896, rk + i * 896 + 224);
        }
        const bool emit = (j8 & 5) == 0 && g == 10 + kq;
        u32x4 ef[7];
        if (emit) {
#pragma unroll
            for (int i = 0; i < 7; ++i) ef[i] = tr8(lds, rk - 224 * h + fblk(i, 0) * 56, rk - 224 * h + fblk(i, 1) * 56);
        }
        float st_[4] = {0.f, 0.f, 0.f, 0.f}, ck = 0.f, ck2 = 0.f;
        f32x16 ka = {};
        if (kvwg) {
            if (j8 == 0) { _Pragma("unroll") for (int i = 0; i < 7; ++i) ka = mfma(ak[i], xf[0][i], ka); }
            else if (j8 == 1) { _Pragma("unroll") for (int i = 0; i < 7; ++i) ka = mfma(ak[i], xf[1][i], ka); }
            else if (j8 == 2) { _Pragma("unroll") for (int i = 0; i < 7; ++i) ka = mfma(xf[0][i], ak[i], ka); }
            else { _Pragma("unroll") for (int i = 0; i < 7; ++i) ka = mfma(xf[1][i], ak[i], ka); }
#pragma unroll
            for (int i = 0; i < 7; ++i) { ck = dot2(ak[i][0], 0x3f803f80u, ck); ck2 = dot2(ak[i][1], 0x3f803f80u, ck2); ck = dot2(ak[i][2], 0x3f803f80u, ck); ck2 = dot2(ak[i][3], 0x3f803f80u, ck2); }
        }
#pragma unroll
        for (int i = 0; i < 7; ++i)
#pragma unroll
            for (int e = 0; e < 4; ++e) { st_[0] = dot2(xf[0][i][e], 0x3f803f80u, st_[0]); st_[1] = dot2(xf[0][i][e], xf[0][i][e], st_[1]);
                                          st_[2] = dot2(xf[1][i][e], 0x3f803f80u, st_[2]); st_[3] = dot2(xf[1][i][e], xf[1][i][e], st_[3]); }
#pragma unroll
        for (int r = 12; r < 16; ++r) ka[r] = (h && !isv) ? 0.f : ka[r];
#pragma unroll
        for (int i = 0; i < 4; ++i) st_[i] = xhalf_sum(st_[i]);
        ck = xhalf_sum(ck + ck2);
        lds_st(lds, F_WK + kq * QK + l16, pack8(ka, 0)); lds_st(lds, F_WK + kq * QK + 1024 + l16, pack8(ka, 1));
        *(f32x2*)(smem + F_WK + kq * QK + 2048 + lane * 8) = (f32x2){h ? st_[2] : st_[0], h ? st_[3] : st_[1]};
        *(float*)(smem + F_WK + kq * QK + 2560 + m * 4) = m < 28 ? ck : 0.f;
        arrive(lds, F_CNT + 8, lane);
        if (kvwg && w == 0) {
            wait_for(lds, F_CNT + 8, 4u);
            const u32 kb = finish(sel, lane, F_WK, F_WK + 2560, QK, j8 & 1, isv ? 2 : 1, OFF_KV + ((2 * g + (j8 & 1)) * 4 + (isv ? 2 : 0)) * 1024);
            VM0();
            stx2((u32x2){token, kb << 16}, rs, lane ? OOB : 0u, OFF_F1 + ((2 * g + (j8 & 1)) * 2 + isv) * 128);
            STAMP_AT(13 + 16);
        }
        if (emit) {
#pragma unroll
            for (int i = 0; i < 7; ++i) stx(m < 28 ? ef[i] : (u32x4){0u, 0u, 0u, 0u}, rs, l16, OFF_WKV + ((j8 >> 1) * 28 + 7 * kq + i) * 1024);
            wait_for(lds, F_CNT + 8, 4u);
            const float* p = (const float*)(smem + F_WK + 2560) + m;
            __builtin_amdgcn_raw_buffer_store_b32(__float_as_uint((p[0] + p[QK / 4]) + (p[QK / 2] + p[3 * QK / 4])), rs, h ? OOB : lane * 4, OFF_CS + (16 + (j8 >> 1)) * 128, 16);
            VM0();
        }
        publish(lane);
        STAMP_AT(15 + 16);
    };
    auto start_b = [&]() {
        FRESH_LANE();
        const int v = w - 4, kq = v;
        u32 rq = F_WQ + (112 * kq + 8 * h + ((lane & 15) >> 2)) * 112 + ((lane >> 4) & 1) * 32 + (lane & 3) * 8; PIN(rq);
        const Sel sel = make_sel(lane);
        const bool emit = g == 2 + kq || g == 6 + kq;
        const int ea = g >= 6;
        u32x4 ef[7];
        {
            u32x4 xf[2][7], aq[2][7];
            x_rows(xf, lane);
            if (JOBAT == 1) asm volatile("s_waitcnt vmcnt(7)" ::: "memory"); else VM0();
            arrive(lds, F_CNT + 32, lane);
            wait_for(lds, F_CNT + 32, 4u);
#pragma unroll
            for (int i = 0; i < 7; ++i) { aq[0][i] = tr8(lds, rq + i * 1792, rq + i * 1792 + 448); aq[1][i] = tr8(lds, rq + i * 1792 + 56, rq + i * 1792 + 504); }
            if (emit) {
#pragma unroll
                for (int i = 0; i < 7; ++i) { const u32 p = rq - 448 * h + ea * 56; ef[i] = tr8(lds, p + fblk(i, 0) * 112, p + fblk(i, 1) * 112); }
            }
            SB();
            const f32x16 zero = {};
            f32x16 qa[4]; float cq[4] = {0.f, 0.f, 0.f, 0.f};
#pragma unroll
            for (int i = 0; i < 7; ++i)
#pragma unroll
                for (int c = 0; c < 4; ++c) qa[c] = mfma(aq[c >> 1][i], xf[c & 1][i], i ? qa[c] : zero);
#pragma unroll
            for (int i = 0; i < 7; ++i)
#pragma unroll
                for (int e = 0; e < 4; ++e) { cq[e & 1] = dot2(aq[0][i][e], 0x3f803f80u, cq[e & 1]); cq[2 + (e & 1)] = dot2(aq[1][i][e], 0x3f803f80u, cq[2 + (e & 1)]); }
#pragma unroll
            for (int c = 0; c < 4; ++c) {
#pragma unroll
                for (int r = 12; r < 16; ++r) qa[c][r] = h ? 0.f : qa[c][r];
                lds_st(lds, F_WQ + kq * QQ + 2 * c * 1024 + l16, pack8(qa[c], 0)); lds_st(lds, F_WQ + kq * QQ + (2 * c + 1) * 1024 + l16, pack8(qa[c], 1));
            }
            const float c0 = xhalf_sum(cq[0] + cq[1]), c1 = xhalf_sum(cq[2] + cq[3]);
            *(float*)(smem + F_WQ + kq * QQ + 8192 + lane * 4) = m < 28 ? (h ? c1 : c0) : 0.f;
            arrive(lds, F_CNT + 12, lane); wait_for(lds, F_CNT + 12, 4u); wait_for(lds, F_CNT + 8, 4u);
            finish(sel, lane, F_WQ + 2 * v * 1024, F_WQ + 8192 + (v >> 1) * 128, QQ, v & 1, 0, F_Q + v * 2048);
            arrive(lds, F_CNT + 28, lane);
        }
        STAMP_AT(13 + 16);
        if (emit) {
#pragma unroll
            for (int i = 0; i < 7; ++i) stx(m < 28 ? ef[i] : (u32x4){0u, 0u, 0u, 0u}, rs, l16, OFF_WQ + ((2 * j8 + ea) * 28 + 7 * kq + i) * 1024);
            const float* p = (const float*)(smem + F_WQ + 8192) + lane;
            __builtin_amdgcn_raw_buffer_store_b32(__float_as_uint((p[0] + p[QQ / 4]) + (p[QQ / 2] + p[3 * QQ / 4])), rs, lane * 4, OFF_CS + 2 * j8 * 128, 16);
        }
        if (JOBAT == 2) job_fetch(in, lds0, b, v, lane);
        VM0();
        arrive(lds, F_CNT + 20, lane); wait_for(lds, F_CNT + 20, 4u);
        if (STGATE) wait_for(lds, F_CNT + 4, 4u);
        { u32x4 a4 = {ARMED, ARMED, ARMED, ARMED}; PIN(a4);
#pragma unroll
          for (int i = 0; i < 5; ++i) stx(a4, rs, (i < 4 || v == 0) ? l16 : OOB, OFF_HO + (b * 17 + 4 * i + v) * 1024); }
        STAMP_AT(14 + 16);
        const int q4 = (lane & 15) >> 2;
        if (isg) {
            const u32 base = F_JOB + ((lane >> 4) & 1) * F_UP + v * 32 + (lane & 3) * 8 + (4 * h + q4) * 128;
            u32x4 f[7]; float cs = 0.f;
#pragma unroll
            for (int i = 0; i < 7; ++i) f[i] = tr8(lds, base + fblk(i, 0) * 128, base + fblk(i, 1) * 128);
#pragma unroll
            for (int i = 0; i < 7; ++i) { stx(f[i], rs, l16, OFF_WGU + (((jb & ~3) + v) * 28 + 7 * (jb & 3) + i) * 1024);
#pragma unroll
                for (int e = 0; e < 4; ++e) cs = dot2(f[i][e], 0x3f803f80u, cs); }
            __builtin_amdgcn_raw_buffer_store_b32(__float_as_uint(xhalf_sum(cs)), rs, h ? OOB : m * 4, OFF_CG + ((jb & 3) * 112 + (jb & ~3) + v) * 128, 16);
        } else if (b < 184) {
            u32x4 f[8];
#pragma unroll
            for (int i = 0; i < 8; ++i) {
                const u32 a0 = F_JOB + (16 * (i & 1) + 4 * h + q4) * 896 + 112 * (v + 4 * (i >> 2)) + 64 * ((i >> 1) & 1) + ((lane >> 4) & 1) * 32 + (lane & 3) * 8;
                f[i] = tr8(lds, a0, a0 + 8 * 896);
            }
            const u32 m24 = m < 24 ? ~0u : 0u, mh = (isd || h == 0) ? ~0u : 0u;
#pragma unroll
            for (int i = 0; i < 8; ++i) {
                const int jm = (v + 4 * (i >> 2)) * 2 + ((i >> 1) & 1), sk = i & 1;
                if (i & 2) f[i] &= m24;
                if (sk) { f[i][2] &= mh; f[i][3] &= mh; }
                stx(f[i], rs, l16, isd ? OFF_WD + (jm * 112 + 2 * jb + sk) * 1024 : OFF_WO + (jm * 32 + 2 * jb + sk) * 1024);
            }
        }
        VM0();
        publish(lane);
        STAMP_AT(15 + 16);
    };

    const u32 po = w < 4 ? OFF_WO + (j8 * 64 + 8 * w) * 1024 : OFF_WD + (j8 * 224 + 28 * (w - 4)) * 1024;
    const u32 l16o = l16;
    auto attend = [&](auto role, u32x4* P) {
        constexpr bool A = decltype(role)::value;
        u32 l16 = l16o;
        if (A) asm volatile("; waves 0-3" : "+v"(l16)); else asm volatile("; waves 4-7" : "+v"(l16));
    u32 kvo = (lay ? OFF_KV2 : OFF_KV) + w * 32 * 1024, pb = po, gb = OFF_WGU + (i16 * 196 + w * 21) * 1024, gl = lds0 + L_GU0 + w * 21 * 1024;
    auto kvld = [&](u32 off) { return ldx(rs, l16, kvo + off); };
    auto first_keys = [&](u32x4 (*kf)[4]) {
#pragma unroll
        for (int i = 0; i < 4; ++i) kf[0][i] = kvld(i * 1024);
        kf[1][0] = kvld(4096);
    };
    auto keys_complete = [&]() {
        for (int sp = 0; !wt.dead; ++sp) {
            u32 mx = 0;
            asm volatile("" ::: "memory");
#pragma unroll 1
            for (int i = 0; i < 32; i += 4) { u32x4 t[4];
#pragma unroll
                for (int e = 0; e < 4; ++e) t[e] = kvld((i + e) * 1024);
                mx = umax(mx, fold_all<2, 4>(t)); }
            if (all_written<2>(mx)) break;
            if (sp > SPIN_CAP) wt.dead = true;
        }
    };
    constexpr bool FIRST = lay ? !A : A;
    constexpr u32 lq = lay ? 0 : F_Q, lkn = lay ? L_KN : F_KN;
    if (FIRST) __builtin_amdgcn_s_setprio(1);
    u32x4 q[4][2], kf[2][4];
    u32 knv;
    if (lay == 0) {
        if (A) {
            const u32 pv = ((l16 >> 4) & 15) * 128 + ((l16 >> 8) & 1) * 8192;
            if (POLLQ) wait_for(lds, F_CNT + 28, 4u);
            for (int sp = 0;; ++sp) {
                asm volatile("" ::: "memory");
                const u32x2 f = __builtin_bit_cast(u32x2, __builtin_amdgcn_raw_buffer_load_b64(rs, pv, OFF_F1 + w * 2048, 16));
                knv = f[1];
                if (__builtin_expect(__all(f[0] == token) || wt.dead, 1)) break;
                if (sp > SPIN_CAP) wt.dead = true;
            }
            asm volatile("" ::: "memory");
            first_keys(kf);
            *(u32*)(smem + lkn + w * 128 + ((l16 >> 2) & 124)) = knv;
            arrive(lds, F_CNT + 4, l16);
        }
        wait_for(lds, F_CNT + 28, 4u); wait_for(lds, F_CNT + 4, 4u);
        if (!A) first_keys(kf);
    } else {
        const u32 pv = ((l16 >> 4) & 7) * 4096 + ((l16 & 128) ? 3072 : 1024 + 32 * 16 + 12);
        for (int sp = 0;; ++sp) {
            asm volatile("" ::: "memory");
            knv = __builtin_amdgcn_raw_buffer_load_b32(rs, pv, kvo, 16);
            SB();
            first_keys(kf);
            asm volatile("" ::: "memory");
            if (__builtin_expect(all_written<2>(fold<2>(umax(knv, fold_all<2, 4>(kf[0])), kf[1][0])) || wt.dead, 1)) break;
            if (sp > SPIN_CAP) wt.dead = true;
        }
        *(u32*)(smem + lkn + (l16 < 128 ? w * 32 : 512) + (l16 >> 2)) = knv << 16;
        BAR();
    }
#pragma unroll
    for (int i = 0; i < 8; ++i) q[i >> 1][i & 1] = lds_ld(lds, lq + i * 1024 + l16);
    knv = *(const u32*)(smem + lkn + (l16 >> 2));
    if (lay == 0) knv = umax(knv, *(const u32*)(smem + lkn + 256 + (l16 >> 2)));
    bool checked = lay == 0;
    {
        FRESH_LANE();
        const float kn = __uint_as_float(wave_umax(knv));
        float sh[4]; bool safe = true;
#pragma unroll
        for (int t = 0; t < 4; ++t) { sh[t] = blo(q[t][1][2]) * kn; safe = safe && (h == 0 || sh[t] <= 48.f); }
        if (__builtin_expect(!__all(safe), 0)) {
            if (lay == 1) { keys_complete(); first_keys(kf); checked = true; }
            float* mx = (float*)(smem + 8192);
            float rm[4] = {-INFINITY, -INFINITY, -INFINITY, -INFINITY};
#pragma unroll
            for (int t = 0; t < 4; ++t) q[t][1][2] = h ? 0u : q[t][1][2];
#pragma unroll 1
            for (int kt = 0; kt < 8; ++kt) {
                const u32x4 k0 = kvld(kt * 4096), k1 = kvld(kt * 4096 + 1024);
#pragma unroll
                for (int t = 0; t < 4; ++t) {
                    f32x16 s = {}; s = mfma(k0, q[t][0], s); s = mfma(k1, q[t][1], s);
#pragma unroll
                    for (int r = 0; r < 16; ++r) rm[t] = fmaxf(rm[t], s[r]);
                }
            }
#pragma unroll
            for (int t = 0; t < 4; ++t) { rm[t] = xhalf_max(rm[t]); if (h == 0) mx[(w * 4 + t) * 32 + m] = rm[t]; }
            __syncthreads();
#pragma unroll
            for (int t = 0; t < 4; ++t) { float v = mx[t * 32 + m];
#pragma unroll
                for (int u = 1; u < 8; ++u) v = fmaxf(v, mx[(u * 4 + t) * 32 + m]);
                sh[t] = v; }
            __syncthreads();
        }
#pragma unroll
        for (int t = 0; t < 4; ++t) {
            const u32 hi = pk(-sh[t], 0.f) & 0xffffu; const u32 lo = pk(-sh[t] - blo(hi), 0.f);
            q[t][1][2] = h ? (hi | (lo << 16)) : q[t][1][2];
        }
    }
    STAMP_AT(1 + 16 * lay);
        f32x16 o[4];
        u32 seen[4] = {}; int spins = 0;
        for (;;) {
        asm volatile("" : "+s"(kvo), "+s"(pb), "+s"(gb), "+s"(gl));
#pragma unroll
        for (int t = 0; t < 4; ++t) o[t] = f32x16{};
        f32x16 sb[3];
        const f32x16 zero = {};
        sb[0] = mfma(kf[0][0], q[0][0], zero); sb[0] = mfma(kf[0][1], q[0][1], sb[0]);
#define EX1(x) do { float t_ = __builtin_amdgcn_exp2f(x); PIN(t_); x = t_; } while (0)
#define EX4(S, i) do { EX1(S[i]); EX1(S[i + 1]); EX1(S[i + 2]); EX1(S[i + 3]); SB(); } while (0)
        SB();
#pragma unroll
        for (int u = 0; u < 32; ++u) {
            const int kt = u >> 2, t = u & 3, un = u + 1, ktn = un >> 2, tn = un & 3, up = u - 1;
            f32x16& cur = sb[u % 3]; f32x16& nxt = sb[un % 3]; f32x16& prv = sb[(u + 2) % 3];
            if (un < 32) { nxt = mfma(kf[ktn & 1][0], q[tn][0], zero); PIN(nxt); }
            SB();
            if (lay == 0 && A && u == U2 - 5 && w == 0) {
#pragma unroll
                for (int i = 0; i < 4; ++i) seen[i] = __builtin_amdgcn_raw_buffer_load_b32(rs, l16 * 2, OFF_F2 + i * 2048, 16);
            }
            if (lay == 0 && A && u == U2 - 1 && w == 0) {
                while (!__all(seen[0] == token && seen[1] == token && seen[2] == token && seen[3] == token) && !wt.dead) {
                    asm volatile("" ::: "memory");
#pragma unroll
                    for (int i = 0; i < 4; ++i) seen[i] = __builtin_amdgcn_raw_buffer_load_b32(rs, l16 * 2, OFF_F2 + i * 2048, 16);
                    if (++spins > SPIN_CAP) wt.dead = true;
                }
                arrive(lds, F_CNT + 24, l16);
            }
            if (lay == 0 && u == (A ? U2 : 26)) wait_for(lds, F_CNT + 24, 1u);
            if (A) {
                constexpr int u0 = lay ? 0 : U2, n = lay ? 3 : 1;
#pragma unroll
                for (int i = n * (u - u0); i >= 0 && i < n * (u - u0) + n && i < 14; ++i) P[i] = ldx(rs, l16, pb + ((i >> 3) * 32 + (i & 7)) * 1024);
            }
            SB(); EX4(cur, 0);
            if (un < 32) { nxt = mfma(kf[ktn & 1][1], q[tn][1], nxt); PIN(nxt); }
            SB(); EX4(cur, 4);
            if (up >= 0) { o[up & 3] = smfmac_hi(kf[(up >> 2) & 1][3], __builtin_shufflevector(prv, prv, 8, 9, 10, 11, 12, 13, 14, 15), o[up & 3]); PIN(o[up & 3]); }
            SB();
            if (t == 3) { if (kt + 2 < 8) kf[kt & 1][0] = kvld((kt + 2) * 4096); }
            else if (kt + 1 < 8) kf[(kt + 1) & 1][t + 1] = kvld(((kt + 1) * 4 + t + 1) * 1024);
            SB(); EX4(cur, 8);
            o[t] = smfmac_hi(kf[kt & 1][2], __builtin_shufflevector(cur, cur, 0, 1, 2, 3, 4, 5, 6, 7), o[t]); PIN(o[t]);
            SB();
            if (A) {
                constexpr int u0 = lay ? 0 : U2, n = lay ? 3 : 2;
#pragma unroll
                for (int p = n * (u - u0); p >= 0 && p < n * (u - u0) + n && p < 21; ++p) dma(rs, l16, gb + p * 1024, gl + p * 1024);
            }
            if (lay == 0 && (HFA ? A : !A) && u >= UH && u < UH + 7 * HS && (u - UH) % HS == 0) {
                u32x4 a4 = {ARMED, ARMED, ARMED, ARMED}; stx(a4, rs, l16, OFF_HF + (g * 224 + j8 * 28 + 4 * ((u - UH) / HS) + (w & 3)) * 1024); }
            if (u >= 26 && !A) {
#pragma unroll
                for (int i = 3 * (u - 26); i < 3 * (u - 26) + 3 && i < 16; ++i) P[i] = ldx(rs, l16, pb + ((i & 1) * 112 + 7 * (((i >> 1) + j8) & 3) + (i >> 3)) * 1024);
            }
            if (u == (lay ? FLIP : FLIP0) && FIRST) __builtin_amdgcn_s_setprio(0);
            if (lay == 0 && u == FLIP1 && !FIRST) __builtin_amdgcn_s_setprio(1);
            SB(); EX4(cur, 12);
        }
        o[3] = smfmac_hi(kf[1][3], __builtin_shufflevector(sb[31 % 3], sb[31 % 3], 8, 9, 10, 11, 12, 13, 14, 15), o[3]);
        if (lay == 0) break;
        float pz = o[0][0];
#pragma unroll
        for (int r = 1; r < 16; ++r) pz += o[0][r];
        if (__builtin_expect(checked || !__any(pz != pz), 1)) break;
        keys_complete(); first_keys(kf); checked = true;
        }
        STAMP_AT(2 + 16 * lay);
#pragma unroll
        for (int c = 0; c < 8; ++c) lds_st(lds, L_RED + (w * 8 + c) * 1024 + l16, pack8(o[c >> 1], c & 1));
    };
    if (w < 4) {
        u32x4 P[16], res[2];
        if (lay == 0) start_a();
        attend(std::true_type{}, P);
        SB();
        P[14] = ldx(rs, l16, po + 38 * 1024); P[15] = ldx(rs, l16, po + 39 * 1024);
        if (lay) { res[0] = ldx(rs, l16, o_x2 + xo0); res[1] = ldx(rs, l16, o_x2 + (xo1 & ~15u)); }
        else {
            FRESH_LANE();
            const uint16_t* r = in.x + (size_t)(32 * ntile + m) * D_ + 56 * j8 + 32 * mt + 4 * h;
            const u32x2 r0 = *(const u32x2*)r, r1 = *(const u32x2*)(r + 8), r2 = *(const u32x2*)(r + 16), r3 = *(const u32x2*)(r + (mt ? 16 : 24));
            res[0] = (u32x4){r0[0], r0[1], r1[0], r1[1]}; res[1] = (u32x4){r2[0], r2[1], r3[0], r3[1]};
        }
        SB();
        const Sel sel = make_sel(l16 >> 4);
        f32x16 acc0 = {}; PIN(acc0);
        BAR();
        STAMP_AT(3 + 16 * lay);
        {
            f32x16 acc = add_tiles<8>(sel, lds, L_RED + 2 * w * 1024 + l16, 8192, acc0);
            BAR();
            const auto sw = __builtin_amdgcn_permlane32_swap(__float_as_uint(acc[12]), __float_as_uint(acc[12]), false, false);
            const float il = __builtin_amdgcn_rcpf(__uint_as_float(sw[1]));
#pragma unroll
            for (int r = 0; r < 16; ++r) acc[r] *= il;
            const u32 oo = OFF_OF + ((g * 8 + j8) * 8 + 2 * w) * 1024;
            stx(mark<lay>(pack8(acc, 0)), rs, l16, oo); stx(mark<lay>(pack8(acc, 1)), rs, l16, oo + 1024);
        }
        STAMP_AT(4 + 16 * lay);
        VM0();
        STAMP_AT(5 + 16 * lay);
        u32x4 xr[2];
        {
            u32x4 ob[16];
            const u32 oo = OFF_OF + (g * 8 + 2 * w) * 8192;
            auto old = [&](int i) { const int e = (i >> 2) * 4 + (i & 1) * 2 + ((i >> 1) & 1); return ldx(rs, l16 + (e & 3) * 1024, oo + (e >> 2) * 4096); };
            gated<lay>(rs, ((l16 >> 4) & 7) * 2048 + 1024, oo, wt, [&]() {
#pragma unroll
                for (int i = 0; i < 10; ++i) ob[i] = old(i); });
#pragma unroll
            for (int i = 10; i < 16; ++i) ob[i] = old(i);
            if (mt) res[1] = (lay && (j8 & 1)) ? (u32x4){res[1][2], res[1][3], 0u, 0u} : (u32x4){res[1][0], res[1][1], 0u, 0u};
            f32x16 y0 = add_tile(sel, res[0], res[1], f32x16{}); PIN(y0);
            f32x16 acc[4] = {};
#pragma unroll
            for (int c = 0; c < 4; ++c) {
                settle<lay, 4>(ob + 4 * c, unwritten<lay>(fold_all<lay, 4>(ob + 4 * c)), wt, [&](int i) { return old(4 * c + i); });
#pragma unroll
                for (int i = 0; i < 4; ++i) { const int ks = 2 * c + (i >> 1), n = i & 1;
                    acc[n] = mfma(P[ks], ob[4 * c + i], acc[n]); acc[2 + n] = mfma(P[8 + ks], ob[4 * c + i], acc[2 + n]); }
            }
            STAMP_AT(6 + 16 * lay);
#pragma unroll
            for (int c = 0; c < 8; ++c) lds_st(lds, (w * 8 + c) * 1024 + l16, pack8(acc[c >> 1], c & 1));
            BAR();
            f32x16 y = add_tiles<4>(sel, lds, 2 * w * 1024 + l16, 8192, y0);
            BAR();
            xr[0] = pack8(y, 0); xr[1] = pack8(y, 1);
            stx(mark<lay>(xr[0]), rs, l16, o_x1 + xo0);
            if (mt) stx2((u32x2){mark1<lay>(xr[1][0]), mark1<lay>(xr[1][1])}, rs, l16, o_x1 + xo1); else stx(mark<lay>(xr[1]), rs, l16, o_x1 + xo1);
            float s1 = 0.f, s2 = 0.f;
#pragma unroll
            for (int r = 0; r < 16; ++r) { s1 += y[r]; s2 = fmaf(y[r], y[r], s2); }
            s1 = xhalf_sum(s1); s2 = xhalf_sum(s2);
            stx2((u32x2){mark1<lay>(__float_as_uint(s1)), mark1<lay>(__float_as_uint(s2))}, rs, l16 < 512 ? l16 : OOB, o_st1 + sto);
        }
        STAMP_AT(7 + 16 * lay);
        {
            __builtin_amdgcn_s_setprio(2);
            FRESH_LANE();
            const int nt = 4 * blk1 + w;
            const u32 xo = o_x1 + nt * 28 * 1024, so = o_st1 + nt * 4096 + 0u, sv = (4 * h) * 512 + m * 16;
            u32x4 x[28], sr[4];
            f32x16 a[4]; const f32x16 zero = {};
            u32x4 ar[6], br[RING];
            u32 lb = l16 + L_GU0, lc = l16 + L_GU0 + 56 * 1024, lcs = L_CS + h * 16; PIN(lb); PIN(lc); PIN(lcs);
            auto gu012 = [&](int tt, int ks) { return tt < 2 ? lds_ld(lds, lb + (tt * 28 + ks) * 1024) : lds_ld(lds, lc + ks * 1024); };
            auto gu36 = [&](int e) { return e < 28 ? lds_ld(lds, l16 + L_GU3 + e * 1024) : e < 56 ? lds_ld(lds, l16 + L_GU4 + (e - 28) * 1024) : lds_ld(lds, lb + (e - 56) * 1024); };
#pragma unroll
            for (int i = 0; i < 6; ++i) ar[i] = gu012(i % 3, i / 3);
            const int pj = lane & 7;
            const u32 pm = 0u - ((lane >> 3) & 1);
            u32 pv = 7168 * (pj >> 1) + ((6144 + 8 * (pj & 1)) & pm) + ((3072 * (pj & 1) + 1024) & ~pm); PIN(pv);
            SB();
            asm volatile("s_waitcnt vmcnt(1)" ::: "memory");
            STAMP_AT(8 + 16 * lay);
            gated<lay>(rs, pv, xo, wt, [&]() {
#pragma unroll
                for (int ks = 0; ks < 10; ++ks) x[ks] = ldx(rs, l16 + (ks & 3) * 1024, xo + (ks >> 2) * 4096); });
            settle<lay, 2>(x, unwritten<lay>(fold_all<lay, 2>(x)), wt, [&](int i) { return ldx(rs, l16, xo + i * 1024); });
            x[10] = ldx(rs, l16 + 2048, xo + 8192); x[11] = ldx(rs, l16 + 3072, xo + 8192);
            float nmean = 0.f;
            SB();
#pragma unroll
            for (int c = 0; c < 14; ++c) {
                u32 mx = 0; u64 bad = 0, bads = 0;
#pragma unroll
                for (int i = 0; i < 6; ++i) {
                    const int ks = 2 * c + i / 3, tt = i % 3, e = ks * 3 + tt;
                    a[tt] = mfma(ar[e % 6], x[ks], ks == 0 ? zero : a[tt]);
                    if (ks + 2 < 28) ar[e % 6] = gu012(tt, ks + 2); else if (i < RING) br[i] = gu36(i);
                    if (tt == 0) { if (ks + 12 < 28) x[ks + 12] = ldx(rs, l16 + (ks & 3) * 1024, xo + ((ks + 12) >> 2) * 4096); else if (ks < 20) sr[ks - 16] = ldx(rs, sv, so + (ks - 16) * 512); }
                    if (c < 13) {
                        if (i == 3) { mx = fold_all<lay, 2>(x + 2 * c + 2); PIN(mx); }
                        if (i == 4) bad = unwritten<lay>(mx);
                        if (c == 12 && i == 1) { mx = fold_all<lay, 4>(sr); PIN(mx); }
                        if (c == 12 && i == 2) bads = unwritten<lay>(mx);
                    } else {
                        if (i == 0) { nmean = (__uint_as_float(sr[0][0]) + __uint_as_float(sr[0][2])) + (__uint_as_float(sr[1][0]) + __uint_as_float(sr[1][2])); PIN(nmean); }
                        if (i == 1) { nmean += (__uint_as_float(sr[2][0]) + __uint_as_float(sr[2][2])) + (__uint_as_float(sr[3][0]) + __uint_as_float(sr[3][2])); PIN(nmean); }
                        if (i == 2) { nmean = xhalf_sum(nmean) * (-1.f / D_); PIN(nmean); }
                    }
                    SB();
                }
                if (c < 13) settle<lay, 2>(x + 2 * c + 2, bad, wt, [&](int i) { return ldx(rs, l16, xo + (2 * c + 2 + i) * 1024); });
                if (c == 12) settle<lay, 4>(sr, bads, wt, [&](int i) { return ldx(rs, sv, so + i * 512); });
                SB();
            }
            STAMP_AT(9 + 16 * lay);
            f32x4 cs[2][4]; float hv[2][8]; u32x4 hp[2];
            auto epi = [&](int z, int e, int k, f32x16& v) {
                if (k < 2) { cs[z][2 * k] = __builtin_bit_cast(f32x4, lds_ld(lds, lcs + e * 128 + (2 * k) * 32)); cs[z][2 * k + 1] = __builtin_bit_cast(f32x4, lds_ld(lds, lcs + e * 128 + (2 * k + 1) * 32)); }
                else if (k >= 3 && k < 11) { const int r = k - 3;
                    float gg = fmaf(nmean, cs[z][r >> 2][r & 3], v[r]), uu = v[r + 8];
                    asm volatile("" : "+v"(gg), "+v"(uu));
                    hv[z][r] = gg * fmaf(nmean, cs[z][2 + (r >> 2)][r & 3], uu); PIN(hv[z][r]); }
                else if (k == 11) { hp[z][0] = mark1<lay>(pk(hv[z][0], hv[z][1])); hp[z][1] = mark1<lay>(pk(hv[z][2], hv[z][3])); PIN(hp[z]); }
                else if (k == 12) { hp[z][2] = mark1<lay>(pk(hv[z][4], hv[z][5])); hp[z][3] = mark1<lay>(pk(hv[z][6], hv[z][7])); PIN(hp[z]); }
                else if (k == 13) stx(hp[z], rs, l16, o_hf + (nt * 112 + 7 * i16 + e) * 1024);
            };
#pragma unroll
            for (int tt = 3; tt < 7; ++tt) {
#pragma unroll
                for (int ks = 0; ks < 28; ++ks) {
                    const int e = (tt - 3) * 28 + ks;
                    a[tt & 3] = mfma(br[e % RING], x[ks], ks == 0 ? zero : a[tt & 3]);
                    if (e == 2) RAWBAR();
                    if (e == 56 - RING) RAWBAR();
                    if (e == 68) RAWBAR();
                    if (e + RING < 112) br[e % RING] = gu36(e + RING);
                    if (tt == 3) epi(ks / 14, ks / 14, ks % 14, a[ks / 14]);
                    else if (tt == 4) epi(ks / 14, 2 + ks / 14, ks % 14, a[2 + ks / 14]);
                    else if (ks >= 1 && ks < 15) epi(0, tt - 1, ks - 1, a[(tt - 1) & 3]);
                    else if (tt == 6 && ks >= 26) epi(0, 6, ks - 26, a[2]);
                    SB();
                }
            }
#pragma unroll
            for (int k = 2; k < 14; ++k) epi(0, 6, k, a[2]);
            __builtin_amdgcn_s_setprio(0);
        }
        STAMP_AT(10 + 16 * lay);
        const Sel sel2 = make_sel(l16 >> 4);
        float r2; f32x16 y0 = {}, xres;
        { FRESH_LANE();
          const u32 so = o_st1 + ntile * 4096, sv = (4 * h) * 512 + m * 16;
          u32x4 sr[4];
#pragma unroll
          for (int i = 0; i < 4; ++i) sr[i] = ldx(rs, sv, so + i * 512);
          settle<lay, 4>(sr, unwritten<lay>(fold_all<lay, 4>(sr)), wt, [&](int i) { return ldx(rs, sv, so + i * 512); });
          float mean, rstd; stats_from(sr, mean, rstd); r2 = rstd * rstd;
#pragma unroll
          for (int i = 0; i < 8; ++i) { xres[2 * i] = blo(xr[i >> 2][i & 3]); xres[2 * i + 1] = bhi(xr[i >> 2][i & 3]); } }
        u32 lpd = L_WD1 + 2 * w * 1024 + l16; PIN(lpd); PIN(r2); PIN(y0); PIN(xres);
        BAR();
        STAMP_AT(11 + 16 * lay);
        {
            f32x16 y = add_tiles<4>(sel2, lds, lpd, 8192, y0);
#pragma unroll
            for (int r = 0; r < 16; ++r) y[r] = fmaf(y[r], r2, xres[r]);
            if (lay == 1) {
                FRESH_LANE();
                const u32x4 p0 = pack8(y, 0), p1 = pack8(y, 1);
                uint16_t* r = out + (size_t)(32 * ntile + m) * D_ + 56 * j8 + 32 * mt + 4 * h;
                *(u32x2*)r = (u32x2){p0[0], p0[1]}; *(u32x2*)(r + 8) = (u32x2){p0[2], p0[3]}; *(u32x2*)(r + 16) = (u32x2){p1[0], p1[1]};
                if (mt == 0) *(u32x2*)(r + 24) = (u32x2){p1[2], p1[3]};
            } else {
            const u32x4 p0 = mark<2>(pack8(y, 0)), p1 = mark<2>(pack8(y, 1));
            stx(p0, rs, l16, o_x2 + xo0);
            if (mt) stx2((u32x2){p1[0], p1[1]}, rs, l16, o_x2 + xo1); else stx(p1, rs, l16, o_x2 + xo1);
            float s1 = 0.f, s2 = 0.f;
#pragma unroll
            for (int r = 0; r < 16; ++r) { s1 += y[r]; s2 = fmaf(y[r], y[r], s2); }
            s1 = xhalf_sum(s1); s2 = xhalf_sum(s2);
            stx2((u32x2){mark1<2>(__float_as_uint(s1)), mark1<2>(__float_as_uint(s2))}, rs, l16 < 512 ? l16 : OOB, o_st2 + sto);
            }
        }
        STAMP_AT(12 + 16 * lay);
        if (lay == 0) asm volatile("s_waitcnt vmcnt(1)" ::: "memory");
    } else {
        u32x4 P[16], P2[16];
        if (lay == 0) start_b();
        attend(std::false_type{}, P);
        if (lay == 0) __builtin_amdgcn_s_setprio(0);
        const int kq = w - 4;
        const u32 gu = OFF_WGU + (i16 * 196 + kq * 7) * 1024, wdo = OFF_WD + (j8 * 224 + 28 * kq) * 1024;
        auto wdf = [&](int i, int t0) { return ldx(rs, l16, wdo + ((i & 1) * 112 + 7 * (((i >> 1) + j8) & 3) + t0 + (i >> 3)) * 1024); };
        u32x4 P3[16];
        auto pre = [&](int i) { if (i < 16) P2[i] = wdf(i, 2); else P3[i - 16] = wdf(i - 16, 5); };
#pragma unroll
        for (int i = 0; i < NA; ++i) pre(i);
        BAR();
        STAMP_AT(3 + 16 * lay);
        if (lay == 0) {
            const u32x4 a4 = {ARMED, ARMED, ARMED, ARMED};
            stx(a4, rs, l16, o_x2 + xo0);
            if (mt) stx2((u32x2){ARMED, ARMED}, rs, l16, o_x2 + xo1); else stx(a4, rs, l16, o_x2 + xo1);
            stx2((u32x2){ARMED, ARMED}, rs, l16 < 512 ? l16 : OOB, o_st2 + sto);
        }
#pragma unroll
        for (int i = NA; i < NA + NB; ++i) pre(i);
        BAR();
#pragma unroll
        for (int i = 0; i < 7; ++i) dma(rs, l16, gu + (84 + i) * 1024, lds0 + L_GU3 + (kq * 7 + i) * 1024);
        if (lay == 0) { float cs = 0.f;
#pragma unroll
          for (int p = 0; p < 4; ++p) cs += __uint_as_float(__builtin_amdgcn_raw_buffer_load_b32(rs, l16 >> 2, OFF_CG + (p * 112 + 7 * i16) * 128 + kq * 224, 16));
          *(float*)(smem + (l16 < 56 * 16 ? L_CS + kq * 224 : L_KN + 512) + (l16 >> 2)) = cs; }
        VM0();
        BAR();
#pragma unroll
        for (int i = NA + NB; i < NA + NB + NC; ++i) pre(i);
        BAR();
#pragma unroll
        for (int i = 0; i < 7; ++i) dma(rs, l16, gu + (112 + i) * 1024, lds0 + L_GU4 + (kq * 7 + i) * 1024);
        __builtin_amdgcn_s_sleep(PS0);
#pragma unroll
        for (int i = NA + NB + NC; i < 32 - ND; ++i) { pre(i); SB(); __builtin_amdgcn_s_sleep(PS1); SB(); }
        asm volatile("s_waitcnt vmcnt(%0)" ::"i"(32 - ND - NA - NB - NC) : "memory");
        RAWBAR();
#pragma unroll
        for (int i = 0; i < 7; ++i) { dma(rs, l16, gu + (140 + i) * 1024, lds0 + L_GU5 + (kq * 7 + i) * 1024); if (DSL) __builtin_amdgcn_s_sleep(DSL); }
#pragma unroll
        for (int i = 0; i < 7; ++i) { dma(rs, l16, gu + (168 + i) * 1024, lds0 + L_GU6 + (kq * 7 + i) * 1024); if (DSL) __builtin_amdgcn_s_sleep(DSL); }
        asm volatile("s_waitcnt vmcnt(7)" ::: "memory");
        RAWBAR();
#pragma unroll
        for (int i = 0; i < 8; ++i) { dma(rs, l16, wdo + ((i & 1) * 112 + 7 * (((i >> 1) + j8) & 3) + 4) * 1024, lds0 + L_WD1 + (kq * 8 + i) * 1024); if (DSL) __builtin_amdgcn_s_sleep(DSL); }
#pragma unroll
        for (int i = 32 - ND; i < 32; ++i) pre(i);
        asm volatile("s_waitcnt vmcnt(%0)" ::"i"(8 + ND) : "memory");
        RAWBAR();
        STAMP_AT(8 + 16 * lay);
        {
            const u32 ho = o_hf + (2 * g * 112 + 28 * kq) * 1024;
            u32 hs[4][2], l16b = l16 + 4096; PIN(l16b);
#pragma unroll
            for (int i = 0; i < 8; ++i) { hs[i >> 1][i & 1] = ho + ((i & 1) * 112 + 7 * (((i >> 1) + j8) & 3)) * 1024; asm volatile("" : "+s"(hs[i >> 1][i & 1])); }
            auto hld = [&](int s, int n) { return SAMEH ? ldx(rs, l16, hs[s & 3][n]) : ldx(rs, (s < 16 ? l16 : l16b) + ((s >> 2) & 3) * 1024, hs[s & 3][n]); };
            u32x4 hb[28][2], al[2][2];
            auto window = [&](int s0, int s1, u64 bad) {
                if (__builtin_expect(bad != 0, 0)) {
                    for (int sp = 0; !wt.dead; ++sp) {
                        asm volatile("" ::: "memory");
#pragma unroll
                        for (int z = s0; z < s1 && z < 28; ++z) { hb[z][0] = hld(z, 0); hb[z][1] = hld(z, 1); }
                        __builtin_amdgcn_s_waitcnt(0x0F70);
                        if (all_written<lay>(fold_all<lay, 2>(hb[s0]))) break;
                        if (sp > SPIN_CAP) wt.dead = true;
                    }
                }
            };
#pragma unroll
            for (int s = 0; s < 5; ++s) { hb[s][0] = hld(s, 0); hb[s][1] = hld(s, 1); }
            SB();
#pragma unroll
            for (int i = 0; i < 16; ++i) lds_st(lds, L_WD2 + (kq * 16 + i) * 1024 + l16, P3[i]);
            SB();
            hb[5][0] = hld(5, 0); hb[5][1] = hld(5, 1);
            u32 lw1 = L_WD1 + kq * 8192 + l16, lw2 = L_WD2 + kq * 16384 + l16; PIN(lw1); PIN(lw2);
            f32x16 acc[4]; const f32x16 zero = {};
            window(0, 6, unwritten<lay>(fold_all<lay, 2>(hb[0])));
            STAMP_AT(9 + 16 * lay);
#pragma unroll
            for (int s = 0; s < 28; ++s) {
                const u32x4 a0 = s < 8 ? P[(2 * s) & 15] : s < 16 ? P2[(2 * s) & 15] : al[s & 1][0], a1 = s < 8 ? P[(2 * s + 1) & 15] : s < 16 ? P2[(2 * s + 1) & 15] : al[s & 1][1];
                const bool two = s >= 1 && s < 1 + DIST - 6;
                const int z = s < 1 ? 6 : two ? 5 + 2 * s : s + DIST;
                acc[0] = mfma(a0, hb[s][0], s == 0 ? zero : acc[0]);
                if (z < 28) hb[z][0] = hld(z, 0);
                if (two) hb[z][1] = hld(z, 1);
                SB();
                acc[2] = mfma(a1, hb[s][0], s == 0 ? zero : acc[2]);
                if (two) { hb[z + 1][0] = hld(z + 1, 0); hb[z + 1][1] = hld(z + 1, 1); } else if (z < 28) hb[z][1] = hld(z, 1);
                if (s + 1 >= 16 && s + 1 < 28) {
                    const u32 lo = s + 1 < 20 ? lw1 + 2 * (s + 1 - 16) * 1024 : lw2 + 2 * (s + 1 - 20) * 1024;
                    al[(s + 1) & 1][0] = lds_ld(lds, lo); al[(s + 1) & 1][1] = lds_ld(lds, lo + 1024);
                }
                SB();
                acc[1] = mfma(a0, hb[s][1], s == 0 ? zero : acc[1]);
                u64 bad = 0;
                if (s + 1 < 28) bad = unwritten<lay>(fold_all<lay, 2>(hb[s + 1]));
                SB();
                acc[3] = mfma(a1, hb[s][1], s == 0 ? zero : acc[3]);
                SB();
                if (s + 1 < 28) window(s + 1, two ? z + 2 : z + 1, bad);
                SB();
            }
            STAMP_AT(10 + 16 * lay);
#pragma unroll
            for (int c = 0; c < 8; ++c) lds_st(lds, L_WD1 + (kq * 8 + c) * 1024 + l16, pack8(acc[c >> 1], c & 1));
        }
        BAR();
        STAMP_AT(11 + 16 * lay);
        if (lay == 0) {
#pragma unroll
        for (int i = 0; i < 14; ++i) dma(rs, l16, OFF_WQ + (j8 * 56 + kq * 14 + i) * 1024, lds0 + L_WQ + (kq * 14 + i) * 1024);
        if (j8 < 4) {
#pragma unroll
            for (int i = 0; i < 7; ++i) dma(rs, l16, OFF_WKV + ((j8 >> 1) * 28 + kq * 7 + i) * 1024, lds0 + L_WKV + (kq * 7 + i) * 1024);
        }
        }
    }
    if (lay == 0) {
        FRESH_LANE();
        const int kq = w >> 1;
        const bool kvjob = j8 < 4 && nn == (j8 & 1), isv = j8 >> 1;
        const u32 xo = o_x2 + (ntile * 28 + 7 * kq) * 1024;
        const int sn = w < 4 ? ntile : 2 * g + (j8 & 1);
        const u32 so = o_st2 + sn * 4096, sv = (4 * h) * 512 + m * 16;
        const bool fin = w < 4 || (w == 4 && j8 < 4);
        const int kind = w < 4 ? 0 : (isv ? 2 : 1), tile = w < 4 ? 2 * j8 + mt : 16 + isv;
        u32x4 x[7], sr[4], wa[21]; f32x4 cs[4];
        asm volatile("s_barrier" ::: "memory");
#pragma unroll
        for (int i = 0; i < 7; ++i) x[i] = ldx(rs, l16, xo + i * 1024);
        asm volatile("s_waitcnt vmcnt(7)\n\ts_barrier" ::: "memory");
        if (fin) {
#pragma unroll
            for (int i = 0; i < 4; ++i) sr[i] = ldx(rs, sv, so + i * 512);
            load_cs(cs, kind, rs, tile, lane);
        }
#pragma unroll
        for (int i = 0; i < 7; ++i) { wa[i] = lds_ld(lds, L_WQ + (7 * kq + i) * 1024 + l16); wa[7 + i] = lds_ld(lds, L_WQ + (28 + 7 * kq + i) * 1024 + l16); }
        if (kvjob) {
#pragma unroll
            for (int i = 0; i < 7; ++i) wa[14 + i] = lds_ld(lds, L_WKV + (7 * kq + i) * 1024 + l16);
        }
        const Sel sel3 = make_sel(l16 >> 4);
        u32 lpe = L_PE + l16; PIN(lpe);
        SB();
        settle<2, 7>(x, unwritten<2>(fold_all<2, 7>(x)), wt, [&](int i) { return ldx(rs, l16, xo + i * 1024); });
        STAMP_AT(13 + 16 * lay);
        const f32x16 zero = {};
        f32x16 qa[2], ka;
        if (kvjob) {
            if (isv) {
#pragma unroll
                for (int i = 0; i < 7; ++i) ka = mfma(x[i], wa[14 + i], i ? ka : zero);
            } else {
#pragma unroll
                for (int i = 0; i < 7; ++i) ka = mfma(wa[14 + i], x[i], i ? ka : zero);
            }
        }
#pragma unroll
        for (int i = 0; i < 7; ++i) qa[0] = mfma(wa[i], x[i], i ? qa[0] : zero);
        if (kvjob) { lds_st(lds, lpe + (kq * 10 + 8) * 1024, pack8(ka, 0)); lds_st(lds, lpe + (kq * 10 + 9) * 1024, pack8(ka, 1)); }
#pragma unroll
        for (int i = 0; i < 7; ++i) qa[1] = mfma(wa[7 + i], x[i], i ? qa[1] : zero);
        float mean = 0.f, rstd = 0.f;
        if (fin) {
            settle<2, 4>(sr, unwritten<2>(fold_all<2, 4>(sr)), wt, [&](int i) { return ldx(rs, sv, so + i * 512); });
            stats_from(sr, mean, rstd);
        }
        PIN(mean); PIN(rstd);
#pragma unroll
        for (int c = 0; c < 4; ++c) lds_st(lds, lpe + (kq * 10 + (c >> 1) * 4 + nn * 2 + (c & 1)) * 1024, pack8(qa[c >> 1], c & 1));
        f32x16 y = {}; PIN(y);
        BAR();
        STAMP_AT(14 + 16 * lay);
        if (fin) {
        y = add_tiles<4>(sel3, lds, lpe + 2 * w * 1024, 10240, y);
        const u32 dst = w < 4 ? w * 2048 : (u32)OFF_KV2 + (sn * 4 + (isv ? 2 : 0)) * 1024;
        qkv_finish<false>(y, kind, mean, rstd, cs, rs, lds, dst, (float*)(smem + L_SC), lane);
        STAMP_AT(15 + 16 * lay);
        if (w == 4) VM0();
        }
        if (w == 4 && j8 >= 4) __builtin_amdgcn_s_sleep(KVSL);
        RAWBAR();
    }
    STAMP_DUMP();
}

__global__ void __launch_bounds__(512) k_main(const uint16_t* x, const uint16_t* wq, const uint16_t* wk, const uint16_t* wv, const uint16_t* wo,
                                              const uint16_t* wg, const uint16_t* wu, const uint16_t* wd, char* ws, uint16_t* out) {
    extern __shared__ __attribute__((aligned(16))) char smem[];
    Wait wt;
    const In in = {x, wq, wk, wv, wo, wg, wu, wd};
    { constexpr int lay = 0; STAMP_INIT(); STAMP_AT(0); }
    const u32 token = first_fetch(in, ws, smem);
    layer<0>(ws, in, out, smem, wt, token);
    if (blockIdx.x == 0 && threadIdx.x == 0) *(volatile u32*)(ws + OFF_EP) = token;
    layer<1>(ws, in, out, smem, wt, token);
}

extern "C" void kernel_launch(void* const* d_in, const int* in_sizes, int n_in, void* d_out, int out_size, void* d_ws, size_t ws_size, hipStream_t stream) {
    (void)hipFuncSetAttribute((const void*)k_main, hipFuncAttributeMaxDynamicSharedMemorySize, L_TOTAL);
    k_main<<<256, 512, L_TOTAL, stream>>>((const uint16_t*)d_in[0], (const uint16_t*)d_in[1], (const uint16_t*)d_in[2], (const uint16_t*)d_in[3], (const uint16_t*)d_in[4],
                                          (const uint16_t*)d_in[6], (const uint16_t*)d_in[7], (const uint16_t*)d_in[8], (char*)d_ws, (uint16_t*)d_out);
}
```
